```python
import torch

def _softmax(x, axis=-1):
    return torch.softmax(x, dim=axis)

def _gelu(x):
    return torch.nn.functional.gelu(x, approximate='tanh')

def _silu(x):
    return torch.nn.functional.silu(x)

def _sigmoid(x):
    return torch.sigmoid(x)

def _layernorm(x, axis=-1, eps=1e-05):
    m = x.mean(dim=axis, keepdim=True)
    v = x.var(dim=axis, keepdim=True, unbiased=False)
    return (x - m) / torch.sqrt(v + eps)

def _scatter_add(tgt, idx, src, axis=0):
    return tgt.clone().index_add_(axis, idx, src)

def _segment_sum(src, seg, n):
    out = torch.zeros((n,) + src.shape[1:], dtype=src.dtype)
    return out.index_add_(0, seg, src)

def _pad(x, pw):
    flat = tuple((v for pair in reversed(pw) for v in pair))
    return torch.nn.functional.pad(x, flat)
kernel_name = 'adaLN_cond__linear_kernel_hedgehog__glu_gated_swiglu__prompt'

def setup_inputs():

    _w = lambda a: (lambda t: t.to(torch.bfloat16) if t.is_floating_point() else t)(torch.as_tensor(a))

    def _A_setup():
        B, S, D, H = (1, 2048, 1024, 32)
        Dh = D // H
        return {'x': _w((lambda *s: torch.randn(*s, dtype=torch.bfloat16) * 0.05)(B, S, D)), 'wqkv': _w((lambda *s: torch.randn(*s, dtype=torch.bfloat16) * 0.4)(D, 3 * D)), 'w_phi': _w((lambda *s: torch.randn(*s, dtype=torch.bfloat16) * 0.4)(H, Dh, Dh) * Dh ** (-0.5)), 'wo': _w((lambda *s: torch.randn(*s, dtype=torch.bfloat16) * 0.1)(D, D))}
    _A_in = _A_setup()

    def _B_setup():
        B, S, D, F = (1, 2048, 1024, 3584)
        return {'x': _w((lambda *s: torch.randn(*s, dtype=torch.bfloat16) * 0.05)(B, S, D)), 'w1': _w((lambda *s: torch.randn(*s, dtype=torch.bfloat16) * 1.6)(D, F) * (2.0 / D) ** 0.5), 'w3': _w((lambda *s: torch.randn(*s, dtype=torch.bfloat16) * 1.6)(D, F) * (2.0 / D) ** 0.5), 'w2': _w((lambda *s: torch.randn(*s, dtype=torch.bfloat16) * 1.6)(F, D) * (2.0 / F) ** 0.5)}
    _B_in = _B_setup()
    return {'A_x': _A_in['x'], 'A_wqkv': _A_in['wqkv'], 'A_w_phi': _A_in['w_phi'], 'A_wo': _A_in['wo'], 'B_x': _B_in['x'], 'B_w1': _B_in['w1'], 'B_w3': _B_in['w3'], 'B_w2': _B_in['w2'], 'c': _w((lambda *s: torch.randn(*s, dtype=torch.bfloat16) * 0.1)(1, 1024) * 8.0), 'w_mod': _w((lambda *s: torch.randn(*s, dtype=torch.bfloat16) * 0.1)(1024, 6 * 1024) * 1024 ** (-0.5) * 8.0)}

def reference(A_x, A_wqkv, A_w_phi, A_wo, B_x, B_w1, B_w3, B_w2, c, w_mod):

    def _A_ref(x, wqkv, w_phi, wo):
        B, S, D = x.shape
        H = 32
        Dh = D // H
        qkv = (lambda x, *d: x.permute(*d))((x @ wqkv).reshape(B, S, 3, H, Dh), 2, 0, 3, 1, 4)
        q, k, v = (qkv[0], qkv[1], qkv[2])
        pq = torch.einsum('bhsd,hdf->bhsf', q, w_phi)
        pk = torch.einsum('bhsd,hdf->bhsf', k, w_phi)
        phi_q = torch.concatenate([_softmax(pq, axis=-1), _softmax(-pq, axis=-1)], axis=-1)
        phi_k = torch.concatenate([_softmax(pk, axis=-1), _softmax(-pk, axis=-1)], axis=-1)
        kv = torch.einsum('bhsf,bhsd->bhfd', phi_k, v)
        z = torch.einsum('bhsf->bhf', phi_k)
        num = torch.einsum('bhqf,bhfd->bhqd', phi_q, kv)
        den = torch.einsum('bhqf,bhf->bhq', phi_q, z)[..., None] + 1e-06
        o = num / den
        return (lambda x, *d: x.permute(*d))(o, 0, 2, 1, 3).reshape(B, S, D) @ wo

    def _B_ref(x, w1, w3, w2):
        gate = torch.einsum('bsd,df->bsf', x, w1)
        up = torch.einsum('bsd,df->bsf', x, w3)
        h = _silu(gate) * up
        out = torch.einsum('bsf,fd->bsd', h, w2)
        return out
    x = A_x
    mod = (_silu(c) @ w_mod).reshape(c.shape[0], 6, 1, -1)
    sa, ca, ga, sb, cb, gb = (mod[:, 0].clone(), mod[:, 1].clone(), mod[:, 2].clone(), mod[:, 3].clone(), mod[:, 4].clone(), mod[:, 5].clone())
    h = _layernorm(x, axis=-1) * (1 + ca) + sa
    x = x + ga * _A_ref(h, A_wqkv, A_w_phi, A_wo)
    h = _layernorm(x, axis=-1) * (1 + cb) + sb
    x = x + gb * _B_ref(h, B_w1, B_w3, B_w2)
    return x
```

```cpp
#include <hip/hip_runtime.h>
#include <hip/hip_bf16.h>

#define K1_PRE0 \
  "s_nop 4\n" \
  "s_mov_b64 s[44:45], %0\n" \
  "s_mov_b32 s46, %1\n" \
  "s_mov_b32 s47, 0x20000\n" \
  "s_and_b32 s45, s45, 0xffff\n" \
  "s_add_u32 m0, %2, 0\n" \
  "s_mov_b32 s53, 0\n" \
  "buffer_load_dwordx4 %4, s[44:47], s53 offen sc1 lds\n" \
  "s_add_u32 m0, %2, 8192\n" \
  "s_mov_b32 s53, 0\n" \
  "buffer_load_dwordx4 %5, s[44:47], s53 offen sc1 lds\n" \
  "s_add_u32 m0, %2, 16384\n" \
  "s_mov_b32 s53, 0\n" \
  "buffer_load_dwordx4 %6, s[44:47], s53 offen sc1 lds\n" \
  "s_add_u32 m0, %2, 24576\n" \
  "s_mov_b32 s53, 24576\n" \
  "buffer_load_dwordx4 %4, s[44:47], s53 offen sc1 lds\n" \
  "s_add_u32 m0, %2, 32768\n" \
  "s_mov_b32 s53, 24576\n" \
  "buffer_load_dwordx4 %5, s[44:47], s53 offen sc1 lds\n" \
  "s_add_u32 m0, %2, 40960\n" \
  "s_mov_b32 s53, 24576\n" \
  "buffer_load_dwordx4 %6, s[44:47], s53 offen sc1 lds\n"
#define K1_NPRE 6
#define K1_PRECLOB "s40", "s41", "s42", "s43", "s44", "s45", "s46", "s47", "s48", "s49", "s50", "s51", "s52", "s53", "scc", "memory"
#define K1_ASM \
  "s_nop 4\n" \
  "s_mov_b64 s[44:45], %5\n" \
  "s_mov_b32 s46, %6\n" \
  "s_mov_b32 s47, 0x20000\n" \
  "s_mov_b64 s[40:41], %3\n" \
  "s_mov_b32 s42, %4\n" \
  "s_mov_b32 s43, 0x20000\n" \
  "s_and_b32 s41, s41, 0xffff\n" \
  "s_and_b32 s45, s45, 0xffff\n" \
  "s_mov_b32 s53, 0\n" \
  "buffer_load_dwordx4 v[84:87], %9, s[40:43], s53 offen offset:0 sc1\n" \
  "s_mov_b32 s53, 0\n" \
  "buffer_load_dwordx4 v[88:91], %9, s[40:43], s53 offen offset:1024 sc1\n" \
  "s_mov_b32 s53, 0\n" \
  "buffer_load_dwordx4 v[92:95], %9, s[40:43], s53 offen offset:2048 sc1\n" \
  "s_mov_b32 s53, 0\n" \
  "buffer_load_dwordx4 v[96:99], %9, s[40:43], s53 offen offset:3072 sc1\n" \
  "s_mov_b32 s53, 4096\n" \
  "buffer_load_dwordx4 v[100:103], %9, s[40:43], s53 offen offset:0 sc1\n" \
  "s_mov_b32 s53, 4096\n" \
  "buffer_load_dwordx4 v[104:107], %9, s[40:43], s53 offen offset:1024 sc1\n" \
  "s_mov_b32 s53, 4096\n" \
  "buffer_load_dwordx4 v[108:111], %9, s[40:43], s53 offen offset:2048 sc1\n" \
  "s_mov_b32 s53, 4096\n" \
  "buffer_load_dwordx4 v[112:115], %9, s[40:43], s53 offen offset:3072 sc1\n" \
  "s_mov_b32 s53, 8192\n" \
  "buffer_load_dwordx4 v[116:119], %9, s[40:43], s53 offen offset:0 sc1\n" \
  "s_mov_b32 s53, 8192\n" \
  "buffer_load_dwordx4 v[120:123], %9, s[40:43], s53 offen offset:1024 sc1\n" \
  "s_mov_b32 s48, 24576\n" \
  "s_mov_b32 s49, 73728\n" \
  "s_mov_b32 s50, 49152\n" \
  "s_add_u32 s51, %7, 73728\n" \
  "s_mov_b32 s52, 2\n" \
  "v_add_u32 v148, 0, %10\n" \
  "s_waitcnt vmcnt(13) lgkmcnt(0)\n" \
  "s_barrier\n" \
  "ds_read_b128 v[52:55], v148 offset:0\n" \
  "ds_read_b128 v[56:59], v148 offset:1024\n" \
  "ds_read_b128 v[60:63], v148 offset:2048\n" \
  "ds_read_b128 v[64:67], v148 offset:3072\n" \
  "s_waitcnt vmcnt(8) lgkmcnt(2)\n" \
  "v_mfma_f32_16x16x32_bf16 v[160:163], v[52:55], v[84:87], 0\n" \
  "ds_read_b128 v[68:71], v148 offset:4096\n" \
  "v_mfma_f32_16x16x32_bf16 v[164:167], v[52:55], v[88:91], 0\n" \
  "v_mfma_f32_16x16x32_bf16 v[168:171], v[56:59], v[84:87], 0\n" \
  "ds_read_b128 v[72:75], v148 offset:5120\n" \
  "v_mfma_f32_16x16x32_bf16 v[172:175], v[56:59], v[88:91], 0\n" \
  "s_waitcnt lgkmcnt(2)\n" \
  "v_mfma_f32_16x16x32_bf16 v[176:179], v[60:63], v[84:87], 0\n" \
  "s_setprio 2\n" \
  "ds_read_b128 v[76:79], v148 offset:6144\n" \
  "v_mfma_f32_16x16x32_bf16 v[180:183], v[60:63], v[88:91], 0\n" \
  "s_mov_b32 s53, 8192\n" \
  "buffer_load_dwordx4 v[124:127], %9, s[40:43], s53 offen offset:2048 sc1\n" \
  "v_mfma_f32_16x16x32_bf16 v[184:187], v[64:67], v[84:87], 0\n" \
  "ds_read_b128 v[80:83], v148 offset:7168\n" \
  "v_mfma_f32_16x16x32_bf16 v[188:191], v[64:67], v[88:91], 0\n" \
  "s_mov_b32 s53, 8192\n" \
  "buffer_load_dwordx4 v[128:131], %9, s[40:43], s53 offen offset:3072 sc1\n" \
  "s_waitcnt lgkmcnt(2)\n" \
  "v_mfma_f32_16x16x32_bf16 v[192:195], v[84:87], v[68:71], 0\n" \
  "ds_read_b128 v[52:55], v148 offset:8192\n" \
  "v_mfma_f32_16x16x32_bf16 v[196:199], v[88:91], v[68:71], 0\n" \
  "v_mfma_f32_16x16x32_bf16 v[200:203], v[84:87], v[72:75], 0\n" \
  "ds_read_b128 v[56:59], v148 offset:9216\n" \
  "v_mfma_f32_16x16x32_bf16 v[204:207], v[88:91], v[72:75], 0\n" \
  "s_waitcnt vmcnt(8) lgkmcnt(2)\n" \
  "v_mfma_f32_16x16x32_bf16 v[160:163], v[76:79], v[92:95], v[160:163]\n" \
  "ds_read_b128 v[60:63], v148 offset:10240\n" \
  "v_mfma_f32_16x16x32_bf16 v[164:167], v[76:79], v[96:99], v[164:167]\n" \
  "v_mfma_f32_16x16x32_bf16 v[168:171], v[80:83], v[92:95], v[168:171]\n" \
  "ds_read_b128 v[64:67], v148 offset:11264\n" \
  "v_mfma_f32_16x16x32_bf16 v[172:175], v[80:83], v[96:99], v[172:175]\n" \
  "s_waitcnt lgkmcnt(2)\n" \
  "v_mfma_f32_16x16x32_bf16 v[176:179], v[52:55], v[92:95], v[176:179]\n" \
  "s_setprio 1\n" \
  "ds_read_b128 v[68:71], v148 offset:12288\n" \
  "v_mfma_f32_16x16x32_bf16 v[180:183], v[52:55], v[96:99], v[180:183]\n" \
  "s_mov_b32 s53, 12288\n" \
  "buffer_load_dwordx4 v[132:135], %9, s[40:43], s53 offen offset:0 sc1\n" \
  "v_mfma_f32_16x16x32_bf16 v[184:187], v[56:59], v[92:95], v[184:187]\n" \
  "ds_read_b128 v[72:75], v148 offset:13312\n" \
  "v_mfma_f32_16x16x32_bf16 v[188:191], v[56:59], v[96:99], v[188:191]\n" \
  "s_mov_b32 s53, 12288\n" \
  "buffer_load_dwordx4 v[136:139], %9, s[40:43], s53 offen offset:1024 sc1\n" \
  "s_waitcnt lgkmcnt(2)\n" \
  "v_mfma_f32_16x16x32_bf16 v[192:195], v[92:95], v[60:63], v[192:195]\n" \
  "ds_read_b128 v[76:79], v148 offset:14336\n" \
  "v_mfma_f32_16x16x32_bf16 v[196:199], v[96:99], v[60:63], v[196:199]\n" \
  "v_mfma_f32_16x16x32_bf16 v[200:203], v[92:95], v[64:67], v[200:203]\n" \
  "ds_read_b128 v[80:83], v148 offset:15360\n" \
  "v_mfma_f32_16x16x32_bf16 v[204:207], v[96:99], v[64:67], v[204:207]\n" \
  "s_waitcnt vmcnt(8) lgkmcnt(2)\n" \
  "v_mfma_f32_16x16x32_bf16 v[160:163], v[68:71], v[100:103], v[160:163]\n" \
  "ds_read_b128 v[52:55], v148 offset:16384\n" \
  "v_mfma_f32_16x16x32_bf16 v[164:167], v[68:71], v[104:107], v[164:167]\n" \
  "s_add_u32 m0, %7, 49152\n" \
  "s_mov_b32 s53, 49152\n" \
  "buffer_load_dwordx4 %11, s[44:47], s53 offen sc1 lds\n" \
  "v_mfma_f32_16x16x32_bf16 v[168:171], v[72:75], v[100:103], v[168:171]\n" \
  "ds_read_b128 v[56:59], v148 offset:17408\n" \
  "v_mfma_f32_16x16x32_bf16 v[172:175], v[72:75], v[104:107], v[172:175]\n" \
  "s_waitcnt lgkmcnt(2)\n" \
  "v_mfma_f32_16x16x32_bf16 v[176:179], v[76:79], v[100:103], v[176:179]\n" \
  "s_setprio 0\n" \
  "ds_read_b128 v[60:63], v148 offset:18432\n" \
  "v_mfma_f32_16x16x32_bf16 v[180:183], v[76:79], v[104:107], v[180:183]\n" \
  "s_mov_b32 s53, 12288\n" \
  "buffer_load_dwordx4 v[140:143], %9, s[40:43], s53 offen offset:2048 sc1\n" \
  "v_mfma_f32_16x16x32_bf16 v[184:187], v[80:83], v[100:103], v[184:187]\n" \
  "ds_read_b128 v[64:67], v148 offset:19456\n" \
  "v_mfma_f32_16x16x32_bf16 v[188:191], v[80:83], v[104:107], v[188:191]\n" \
  "s_mov_b32 s53, 12288\n" \
  "buffer_load_dwordx4 v[144:147], %9, s[40:43], s53 offen offset:3072 sc1\n" \
  "s_waitcnt lgkmcnt(2)\n" \
  "v_mfma_f32_16x16x32_bf16 v[192:195], v[100:103], v[52:55], v[192:195]\n" \
  "ds_read_b128 v[68:71], v148 offset:20480\n" \
  "v_mfma_f32_16x16x32_bf16 v[196:199], v[104:107], v[52:55], v[196:199]\n" \
  "v_mfma_f32_16x16x32_bf16 v[200:203], v[100:103], v[56:59], v[200:203]\n" \
  "ds_read_b128 v[72:75], v148 offset:21504\n" \
  "v_mfma_f32_16x16x32_bf16 v[204:207], v[104:107], v[56:59], v[204:207]\n" \
  "s_waitcnt vmcnt(9) lgkmcnt(2)\n" \
  "v_mfma_f32_16x16x32_bf16 v[160:163], v[60:63], v[108:111], v[160:163]\n" \
  "ds_read_b128 v[76:79], v148 offset:22528\n" \
  "v_mfma_f32_16x16x32_bf16 v[164:167], v[60:63], v[112:115], v[164:167]\n" \
  "v_mfma_f32_16x16x32_bf16 v[168:171], v[64:67], v[108:111], v[168:171]\n" \
  "ds_read_b128 v[80:83], v148 offset:23552\n" \
  "v_mfma_f32_16x16x32_bf16 v[172:175], v[64:67], v[112:115], v[172:175]\n" \
  "s_waitcnt lgkmcnt(2)\n" \
  "v_mfma_f32_16x16x32_bf16 v[176:179], v[68:71], v[108:111], v[176:179]\n" \
  "v_add_u32 v148, 24576, %10\n" \
  "s_waitcnt vmcnt(17)\n" \
  "s_barrier\n" \
  "s_setprio 3\n" \
  "ds_read_b128 v[52:55], v148 offset:0\n" \
  "v_mfma_f32_16x16x32_bf16 v[180:183], v[68:71], v[112:115], v[180:183]\n" \
  "s_add_u32 m0, %7, 57344\n" \
  "s_mov_b32 s53, 49152\n" \
  "buffer_load_dwordx4 %12, s[44:47], s53 offen sc1 lds\n" \
  "s_mov_b32 s53, 16384\n" \
  "buffer_load_dwordx4 v[84:87], %9, s[40:43], s53 offen offset:0 sc1\n" \
  "v_mfma_f32_16x16x32_bf16 v[184:187], v[72:75], v[108:111], v[184:187]\n" \
  "ds_read_b128 v[56:59], v148 offset:1024\n" \
  "v_mfma_f32_16x16x32_bf16 v[188:191], v[72:75], v[112:115], v[188:191]\n" \
  "s_mov_b32 s53, 16384\n" \
  "buffer_load_dwordx4 v[88:91], %9, s[40:43], s53 offen offset:1024 sc1\n" \
  "s_waitcnt lgkmcnt(2)\n" \
  "v_mfma_f32_16x16x32_bf16 v[192:195], v[108:111], v[76:79], v[192:195]\n" \
  "ds_read_b128 v[60:63], v148 offset:2048\n" \
  "v_mfma_f32_16x16x32_bf16 v[196:199], v[112:115], v[76:79], v[196:199]\n" \
  "v_mfma_f32_16x16x32_bf16 v[200:203], v[108:111], v[80:83], v[200:203]\n" \
  "ds_read_b128 v[64:67], v148 offset:3072\n" \
  "v_mfma_f32_16x16x32_bf16 v[204:207], v[112:115], v[80:83], v[204:207]\n" \
  "s_waitcnt vmcnt(10) lgkmcnt(2)\n" \
  "v_mfma_f32_16x16x32_bf16 v[160:163], v[52:55], v[116:119], v[160:163]\n" \
  "ds_read_b128 v[68:71], v148 offset:4096\n" \
  "v_mfma_f32_16x16x32_bf16 v[164:167], v[52:55], v[120:123], v[164:167]\n" \
  "v_mfma_f32_16x16x32_bf16 v[168:171], v[56:59], v[116:119], v[168:171]\n" \
  "ds_read_b128 v[72:75], v148 offset:5120\n" \
  "v_mfma_f32_16x16x32_bf16 v[172:175], v[56:59], v[120:123], v[172:175]\n" \
  "s_waitcnt lgkmcnt(2)\n" \
  "v_mfma_f32_16x16x32_bf16 v[176:179], v[60:63], v[116:119], v[176:179]\n" \
  "s_setprio 2\n" \
  "ds_read_b128 v[76:79], v148 offset:6144\n" \
  "v_mfma_f32_16x16x32_bf16 v[180:183], v[60:63], v[120:123], v[180:183]\n" \
  "s_mov_b32 s53, 16384\n" \
  "buffer_load_dwordx4 v[92:95], %9, s[40:43], s53 offen offset:2048 sc1\n" \
  "v_mfma_f32_16x16x32_bf16 v[184:187], v[64:67], v[116:119], v[184:187]\n" \
  "ds_read_b128 v[80:83], v148 offset:7168\n" \
  "v_mfma_f32_16x16x32_bf16 v[188:191], v[64:67], v[120:123], v[188:191]\n" \
  "s_mov_b32 s53, 16384\n" \
  "buffer_load_dwordx4 v[96:99], %9, s[40:43], s53 offen offset:3072 sc1\n" \
  "s_waitcnt lgkmcnt(2)\n" \
  "v_mfma_f32_16x16x32_bf16 v[192:195], v[116:119], v[68:71], v[192:195]\n" \
  "ds_read_b128 v[52:55], v148 offset:8192\n" \
  "v_mfma_f32_16x16x32_bf16 v[196:199], v[120:123], v[68:71], v[196:199]\n" \
  "s_add_u32 m0, %7, 65536\n" \
  "s_mov_b32 s53, 49152\n" \
  "buffer_load_dwordx4 %13, s[44:47], s53 offen sc1 lds\n" \
  "v_mfma_f32_16x16x32_bf16 v[200:203], v[116:119], v[72:75], v[200:203]\n" \
  "ds_read_b128 v[56:59], v148 offset:9216\n" \
  "v_mfma_f32_16x16x32_bf16 v[204:207], v[120:123], v[72:75], v[204:207]\n" \
  "s_waitcnt vmcnt(11) lgkmcnt(2)\n" \
  "v_mfma_f32_16x16x32_bf16 v[160:163], v[76:79], v[124:127], v[160:163]\n" \
  "ds_read_b128 v[60:63], v148 offset:10240\n" \
  "v_mfma_f32_16x16x32_bf16 v[164:167], v[76:79], v[128:131], v[164:167]\n" \
  "v_mfma_f32_16x16x32_bf16 v[168:171], v[80:83], v[124:127], v[168:171]\n" \
  "ds_read_b128 v[64:67], v148 offset:11264\n" \
  "v_mfma_f32_16x16x32_bf16 v[172:175], v[80:83], v[128:131], v[172:175]\n" \
  "s_waitcnt lgkmcnt(2)\n" \
  "v_mfma_f32_16x16x32_bf16 v[176:179], v[52:55], v[124:127], v[176:179]\n" \
  "s_setprio 1\n" \
  "ds_read_b128 v[68:71], v148 offset:12288\n" \
  "v_mfma_f32_16x16x32_bf16 v[180:183], v[52:55], v[128:131], v[180:183]\n" \
  "s_mov_b32 s53, 20480\n" \
  "buffer_load_dwordx4 v[100:103], %9, s[40:43], s53 offen offset:0 sc1\n" \
  "v_mfma_f32_16x16x32_bf16 v[184:187], v[56:59], v[124:127], v[184:187]\n" \
  "ds_read_b128 v[72:75], v148 offset:13312\n" \
  "v_mfma_f32_16x16x32_bf16 v[188:191], v[56:59], v[128:131], v[188:191]\n" \
  "s_mov_b32 s53, 20480\n" \
  "buffer_load_dwordx4 v[104:107], %9, s[40:43], s53 offen offset:1024 sc1\n" \
  "s_waitcnt lgkmcnt(2)\n" \
  "v_mfma_f32_16x16x32_bf16 v[192:195], v[124:127], v[60:63], v[192:195]\n" \
  "ds_read_b128 v[76:79], v148 offset:14336\n" \
  "v_mfma_f32_16x16x32_bf16 v[196:199], v[128:131], v[60:63], v[196:199]\n" \
  "v_mfma_f32_16x16x32_bf16 v[200:203], v[124:127], v[64:67], v[200:203]\n" \
  "ds_read_b128 v[80:83], v148 offset:15360\n" \
  "v_mfma_f32_16x16x32_bf16 v[204:207], v[128:131], v[64:67], v[204:207]\n" \
  "s_waitcnt vmcnt(11) lgkmcnt(2)\n" \
  "v_mfma_f32_16x16x32_bf16 v[160:163], v[68:71], v[132:135], v[160:163]\n" \
  "ds_read_b128 v[52:55], v148 offset:16384\n" \
  "v_mfma_f32_16x16x32_bf16 v[164:167], v[68:71], v[136:139], v[164:167]\n" \
  "s_add_u32 m0, %7, 73728\n" \
  "s_mov_b32 s53, 73728\n" \
  "buffer_load_dwordx4 %11, s[44:47], s53 offen sc1 lds\n" \
  "v_mfma_f32_16x16x32_bf16 v[168:171], v[72:75], v[132:135], v[168:171]\n" \
  "ds_read_b128 v[56:59], v148 offset:17408\n" \
  "v_mfma_f32_16x16x32_bf16 v[172:175], v[72:75], v[136:139], v[172:175]\n" \
  "s_waitcnt lgkmcnt(2)\n" \
  "v_mfma_f32_16x16x32_bf16 v[176:179], v[76:79], v[132:135], v[176:179]\n" \
  "s_setprio 0\n" \
  "ds_read_b128 v[60:63], v148 offset:18432\n" \
  "v_mfma_f32_16x16x32_bf16 v[180:183], v[76:79], v[136:139], v[180:183]\n" \
  "s_mov_b32 s53, 20480\n" \
  "buffer_load_dwordx4 v[108:111], %9, s[40:43], s53 offen offset:2048 sc1\n" \
  "v_mfma_f32_16x16x32_bf16 v[184:187], v[80:83], v[132:135], v[184:187]\n" \
  "ds_read_b128 v[64:67], v148 offset:19456\n" \
  "v_mfma_f32_16x16x32_bf16 v[188:191], v[80:83], v[136:139], v[188:191]\n" \
  "s_mov_b32 s53, 20480\n" \
  "buffer_load_dwordx4 v[112:115], %9, s[40:43], s53 offen offset:3072 sc1\n" \
  "s_waitcnt lgkmcnt(2)\n" \
  "v_mfma_f32_16x16x32_bf16 v[192:195], v[132:135], v[52:55], v[192:195]\n" \
  "ds_read_b128 v[68:71], v148 offset:20480\n" \
  "v_mfma_f32_16x16x32_bf16 v[196:199], v[136:139], v[52:55], v[196:199]\n" \
  "v_mfma_f32_16x16x32_bf16 v[200:203], v[132:135], v[56:59], v[200:203]\n" \
  "ds_read_b128 v[72:75], v148 offset:21504\n" \
  "v_mfma_f32_16x16x32_bf16 v[204:207], v[136:139], v[56:59], v[204:207]\n" \
  "s_waitcnt vmcnt(11) lgkmcnt(2)\n" \
  "v_mfma_f32_16x16x32_bf16 v[160:163], v[60:63], v[140:143], v[160:163]\n" \
  "ds_read_b128 v[76:79], v148 offset:22528\n" \
  "v_mfma_f32_16x16x32_bf16 v[164:167], v[60:63], v[144:147], v[164:167]\n" \
  "v_mfma_f32_16x16x32_bf16 v[168:171], v[64:67], v[140:143], v[168:171]\n" \
  "ds_read_b128 v[80:83], v148 offset:23552\n" \
  "v_mfma_f32_16x16x32_bf16 v[172:175], v[64:67], v[144:147], v[172:175]\n" \
  "s_waitcnt lgkmcnt(2)\n" \
  "v_mfma_f32_16x16x32_bf16 v[176:179], v[68:71], v[140:143], v[176:179]\n" \
  "v_add_u32 v148, 49152, %10\n" \
  "s_waitcnt vmcnt(5)\n" \
  "s_barrier\n" \
  "s_setprio 3\n" \
  "ds_read_b128 v[52:55], v148 offset:0\n" \
  "v_mfma_f32_16x16x32_bf16 v[180:183], v[68:71], v[144:147], v[180:183]\n" \
  "s_add_u32 m0, %7, 81920\n" \
  "s_mov_b32 s53, 73728\n" \
  "buffer_load_dwordx4 %12, s[44:47], s53 offen sc1 lds\n" \
  "s_mov_b32 s53, 24576\n" \
  "buffer_load_dwordx4 v[116:119], %9, s[40:43], s53 offen offset:0 sc1\n" \
  "v_mfma_f32_16x16x32_bf16 v[184:187], v[72:75], v[140:143], v[184:187]\n" \
  "ds_read_b128 v[56:59], v148 offset:1024\n" \
  "v_mfma_f32_16x16x32_bf16 v[188:191], v[72:75], v[144:147], v[188:191]\n" \
  "s_mov_b32 s53, 24576\n" \
  "buffer_load_dwordx4 v[120:123], %9, s[40:43], s53 offen offset:1024 sc1\n" \
  "s_waitcnt lgkmcnt(2)\n" \
  "v_mfma_f32_16x16x32_bf16 v[192:195], v[140:143], v[76:79], v[192:195]\n" \
  "ds_read_b128 v[60:63], v148 offset:2048\n" \
  "v_mfma_f32_16x16x32_bf16 v[196:199], v[144:147], v[76:79], v[196:199]\n" \
  "v_mfma_f32_16x16x32_bf16 v[200:203], v[140:143], v[80:83], v[200:203]\n" \
  "ds_read_b128 v[64:67], v148 offset:3072\n" \
  "v_mfma_f32_16x16x32_bf16 v[204:207], v[144:147], v[80:83], v[204:207]\n" \
  "Lloop_%=:\n" \
  "s_waitcnt vmcnt(11) lgkmcnt(2)\n" \
  "v_mfma_f32_16x16x32_bf16 v[160:163], v[52:55], v[84:87], v[160:163]\n" \
  "ds_read_b128 v[68:71], v148 offset:4096\n" \
  "v_mfma_f32_16x16x32_bf16 v[164:167], v[52:55], v[88:91], v[164:167]\n" \
  "v_mfma_f32_16x16x32_bf16 v[168:171], v[56:59], v[84:87], v[168:171]\n" \
  "ds_read_b128 v[72:75], v148 offset:5120\n" \
  "v_mfma_f32_16x16x32_bf16 v[172:175], v[56:59], v[88:91], v[172:175]\n" \
  "s_waitcnt lgkmcnt(2)\n" \
  "v_mfma_f32_16x16x32_bf16 v[176:179], v[60:63], v[84:87], v[176:179]\n" \
  "s_setprio 2\n" \
  "ds_read_b128 v[76:79], v148 offset:6144\n" \
  "v_mfma_f32_16x16x32_bf16 v[180:183], v[60:63], v[88:91], v[180:183]\n" \
  "buffer_load_dwordx4 v[124:127], %9, s[40:43], s48 offen offset:2048 sc1\n" \
  "v_mfma_f32_16x16x32_bf16 v[184:187], v[64:67], v[84:87], v[184:187]\n" \
  "ds_read_b128 v[80:83], v148 offset:7168\n" \
  "v_mfma_f32_16x16x32_bf16 v[188:191], v[64:67], v[88:91], v[188:191]\n" \
  "buffer_load_dwordx4 v[128:131], %9, s[40:43], s48 offen offset:3072 sc1\n" \
  "s_waitcnt lgkmcnt(2)\n" \
  "v_mfma_f32_16x16x32_bf16 v[192:195], v[84:87], v[68:71], v[192:195]\n" \
  "ds_read_b128 v[52:55], v148 offset:8192\n" \
  "v_mfma_f32_16x16x32_bf16 v[196:199], v[88:91], v[68:71], v[196:199]\n" \
  "s_add_u32 m0, s51, 16384\n" \
  "s_nop 0\n" \
  "buffer_load_dwordx4 %13, s[44:47], s49 offen sc1 lds\n" \
  "s_add_u32 s49, s49, 24576\n" \
  "s_add_u32 s51, s51, 24576\n" \
  "s_sub_u32 s53, s51, %7\n" \
  "s_cmp_eq_u32 s53, 98304\n" \
  "s_cselect_b32 s51, %7, s51\n" \
  "v_mfma_f32_16x16x32_bf16 v[200:203], v[84:87], v[72:75], v[200:203]\n" \
  "ds_read_b128 v[56:59], v148 offset:9216\n" \
  "v_mfma_f32_16x16x32_bf16 v[204:207], v[88:91], v[72:75], v[204:207]\n" \
  "s_waitcnt vmcnt(12) lgkmcnt(2)\n" \
  "v_mfma_f32_16x16x32_bf16 v[160:163], v[76:79], v[92:95], v[160:163]\n" \
  "ds_read_b128 v[60:63], v148 offset:10240\n" \
  "v_mfma_f32_16x16x32_bf16 v[164:167], v[76:79], v[96:99], v[164:167]\n" \
  "v_mfma_f32_16x16x32_bf16 v[168:171], v[80:83], v[92:95], v[168:171]\n" \
  "ds_read_b128 v[64:67], v148 offset:11264\n" \
  "v_mfma_f32_16x16x32_bf16 v[172:175], v[80:83], v[96:99], v[172:175]\n" \
  "s_waitcnt lgkmcnt(2)\n" \
  "v_mfma_f32_16x16x32_bf16 v[176:179], v[52:55], v[92:95], v[176:179]\n" \
  "s_setprio 1\n" \
  "ds_read_b128 v[68:71], v148 offset:12288\n" \
  "v_mfma_f32_16x16x32_bf16 v[180:183], v[52:55], v[96:99], v[180:183]\n" \
  "s_add_u32 s53, s48, 4096\n" \
  "buffer_load_dwordx4 v[132:135], %9, s[40:43], s53 offen offset:0 sc1\n" \
  "v_mfma_f32_16x16x32_bf16 v[184:187], v[56:59], v[92:95], v[184:187]\n" \
  "ds_read_b128 v[72:75], v148 offset:13312\n" \
  "v_mfma_f32_16x16x32_bf16 v[188:191], v[56:59], v[96:99], v[188:191]\n" \
  "s_add_u32 s53, s48, 4096\n" \
  "buffer_load_dwordx4 v[136:139], %9, s[40:43], s53 offen offset:1024 sc1\n" \
  "s_waitcnt lgkmcnt(2)\n" \
  "v_mfma_f32_16x16x32_bf16 v[192:195], v[92:95], v[60:63], v[192:195]\n" \
  "ds_read_b128 v[76:79], v148 offset:14336\n" \
  "v_mfma_f32_16x16x32_bf16 v[196:199], v[96:99], v[60:63], v[196:199]\n" \
  "v_mfma_f32_16x16x32_bf16 v[200:203], v[92:95], v[64:67], v[200:203]\n" \
  "ds_read_b128 v[80:83], v148 offset:15360\n" \
  "v_mfma_f32_16x16x32_bf16 v[204:207], v[96:99], v[64:67], v[204:207]\n" \
  "s_waitcnt vmcnt(11) lgkmcnt(2)\n" \
  "v_mfma_f32_16x16x32_bf16 v[160:163], v[68:71], v[100:103], v[160:163]\n" \
  "ds_read_b128 v[52:55], v148 offset:16384\n" \
  "v_mfma_f32_16x16x32_bf16 v[164:167], v[68:71], v[104:107], v[164:167]\n" \
  "s_add_u32 m0, s51, 0\n" \
  "s_nop 0\n" \
  "buffer_load_dwordx4 %11, s[44:47], s49 offen sc1 lds\n" \
  "v_mfma_f32_16x16x32_bf16 v[168:171], v[72:75], v[100:103], v[168:171]\n" \
  "ds_read_b128 v[56:59], v148 offset:17408\n" \
  "v_mfma_f32_16x16x32_bf16 v[172:175], v[72:75], v[104:107], v[172:175]\n" \
  "s_waitcnt lgkmcnt(2)\n" \
  "v_mfma_f32_16x16x32_bf16 v[176:179], v[76:79], v[100:103], v[176:179]\n" \
  "s_setprio 0\n" \
  "ds_read_b128 v[60:63], v148 offset:18432\n" \
  "v_mfma_f32_16x16x32_bf16 v[180:183], v[76:79], v[104:107], v[180:183]\n" \
  "s_add_u32 s53, s48, 4096\n" \
  "buffer_load_dwordx4 v[140:143], %9, s[40:43], s53 offen offset:2048 sc1\n" \
  "v_mfma_f32_16x16x32_bf16 v[184:187], v[80:83], v[100:103], v[184:187]\n" \
  "ds_read_b128 v[64:67], v148 offset:19456\n" \
  "v_mfma_f32_16x16x32_bf16 v[188:191], v[80:83], v[104:107], v[188:191]\n" \
  "s_add_u32 s53, s48, 4096\n" \
  "buffer_load_dwordx4 v[144:147], %9, s[40:43], s53 offen offset:3072 sc1\n" \
  "s_add_u32 s48, s48, 8192\n" \
  "s_waitcnt lgkmcnt(2)\n" \
  "v_mfma_f32_16x16x32_bf16 v[192:195], v[100:103], v[52:55], v[192:195]\n" \
  "ds_read_b128 v[68:71], v148 offset:20480\n" \
  "v_mfma_f32_16x16x32_bf16 v[196:199], v[104:107], v[52:55], v[196:199]\n" \
  "v_mfma_f32_16x16x32_bf16 v[200:203], v[100:103], v[56:59], v[200:203]\n" \
  "ds_read_b128 v[72:75], v148 offset:21504\n" \
  "v_mfma_f32_16x16x32_bf16 v[204:207], v[104:107], v[56:59], v[204:207]\n" \
  "s_waitcnt vmcnt(11) lgkmcnt(2)\n" \
  "v_mfma_f32_16x16x32_bf16 v[160:163], v[60:63], v[108:111], v[160:163]\n" \
  "ds_read_b128 v[76:79], v148 offset:22528\n" \
  "v_mfma_f32_16x16x32_bf16 v[164:167], v[60:63], v[112:115], v[164:167]\n" \
  "v_mfma_f32_16x16x32_bf16 v[168:171], v[64:67], v[108:111], v[168:171]\n" \
  "ds_read_b128 v[80:83], v148 offset:23552\n" \
  "v_mfma_f32_16x16x32_bf16 v[172:175], v[64:67], v[112:115], v[172:175]\n" \
  "s_waitcnt lgkmcnt(2)\n" \
  "v_mfma_f32_16x16x32_bf16 v[176:179], v[68:71], v[108:111], v[176:179]\n" \
  "s_add_u32 s50, s50, 24576\n" \
  "s_cmp_eq_u32 s50, 98304\n" \
  "s_cselect_b32 s50, 0, s50\n" \
  "v_add_u32 v148, s50, %10\n" \
  "s_waitcnt vmcnt(5)\n" \
  "s_barrier\n" \
  "s_setprio 3\n" \
  "ds_read_b128 v[52:55], v148 offset:0\n" \
  "v_mfma_f32_16x16x32_bf16 v[180:183], v[68:71], v[112:115], v[180:183]\n" \
  "s_add_u32 m0, s51, 8192\n" \
  "s_nop 0\n" \
  "buffer_load_dwordx4 %12, s[44:47], s49 offen sc1 lds\n" \
  "buffer_load_dwordx4 v[84:87], %9, s[40:43], s48 offen offset:0 sc1\n" \
  "v_mfma_f32_16x16x32_bf16 v[184:187], v[72:75], v[108:111], v[184:187]\n" \
  "ds_read_b128 v[56:59], v148 offset:1024\n" \
  "v_mfma_f32_16x16x32_bf16 v[188:191], v[72:75], v[112:115], v[188:191]\n" \
  "buffer_load_dwordx4 v[88:91], %9, s[40:43], s48 offen offset:1024 sc1\n" \
  "s_waitcnt lgkmcnt(2)\n" \
  "v_mfma_f32_16x16x32_bf16 v[192:195], v[108:111], v[76:79], v[192:195]\n" \
  "ds_read_b128 v[60:63], v148 offset:2048\n" \
  "v_mfma_f32_16x16x32_bf16 v[196:199], v[112:115], v[76:79], v[196:199]\n" \
  "v_mfma_f32_16x16x32_bf16 v[200:203], v[108:111], v[80:83], v[200:203]\n" \
  "ds_read_b128 v[64:67], v148 offset:3072\n" \
  "v_mfma_f32_16x16x32_bf16 v[204:207], v[112:115], v[80:83], v[204:207]\n" \
  "s_waitcnt vmcnt(11) lgkmcnt(2)\n" \
  "v_mfma_f32_16x16x32_bf16 v[160:163], v[52:55], v[116:119], v[160:163]\n" \
  "ds_read_b128 v[68:71], v148 offset:4096\n" \
  "v_mfma_f32_16x16x32_bf16 v[164:167], v[52:55], v[120:123], v[164:167]\n" \
  "v_mfma_f32_16x16x32_bf16 v[168:171], v[56:59], v[116:119], v[168:171]\n" \
  "ds_read_b128 v[72:75], v148 offset:5120\n" \
  "v_mfma_f32_16x16x32_bf16 v[172:175], v[56:59], v[120:123], v[172:175]\n" \
  "s_waitcnt lgkmcnt(2)\n" \
  "v_mfma_f32_16x16x32_bf16 v[176:179], v[60:63], v[116:119], v[176:179]\n" \
  "s_setprio 2\n" \
  "ds_read_b128 v[76:79], v148 offset:6144\n" \
  "v_mfma_f32_16x16x32_bf16 v[180:183], v[60:63], v[120:123], v[180:183]\n" \
  "buffer_load_dwordx4 v[92:95], %9, s[40:43], s48 offen offset:2048 sc1\n" \
  "v_mfma_f32_16x16x32_bf16 v[184:187], v[64:67], v[116:119], v[184:187]\n" \
  "ds_read_b128 v[80:83], v148 offset:7168\n" \
  "v_mfma_f32_16x16x32_bf16 v[188:191], v[64:67], v[120:123], v[188:191]\n" \
  "buffer_load_dwordx4 v[96:99], %9, s[40:43], s48 offen offset:3072 sc1\n" \
  "s_waitcnt lgkmcnt(2)\n" \
  "v_mfma_f32_16x16x32_bf16 v[192:195], v[116:119], v[68:71], v[192:195]\n" \
  "ds_read_b128 v[52:55], v148 offset:8192\n" \
  "v_mfma_f32_16x16x32_bf16 v[196:199], v[120:123], v[68:71], v[196:199]\n" \
  "s_add_u32 m0, s51, 16384\n" \
  "s_nop 0\n" \
  "buffer_load_dwordx4 %13, s[44:47], s49 offen sc1 lds\n" \
  "s_add_u32 s49, s49, 24576\n" \
  "s_add_u32 s51, s51, 24576\n" \
  "s_sub_u32 s53, s51, %7\n" \
  "s_cmp_eq_u32 s53, 98304\n" \
  "s_cselect_b32 s51, %7, s51\n" \
  "v_mfma_f32_16x16x32_bf16 v[200:203], v[116:119], v[72:75], v[200:203]\n" \
  "ds_read_b128 v[56:59], v148 offset:9216\n" \
  "v_mfma_f32_16x16x32_bf16 v[204:207], v[120:123], v[72:75], v[204:207]\n" \
  "s_waitcnt vmcnt(12) lgkmcnt(2)\n" \
  "v_mfma_f32_16x16x32_bf16 v[160:163], v[76:79], v[124:127], v[160:163]\n" \
  "ds_read_b128 v[60:63], v148 offset:10240\n" \
  "v_mfma_f32_16x16x32_bf16 v[164:167], v[76:79], v[128:131], v[164:167]\n" \
  "v_mfma_f32_16x16x32_bf16 v[168:171], v[80:83], v[124:127], v[168:171]\n" \
  "ds_read_b128 v[64:67], v148 offset:11264\n" \
  "v_mfma_f32_16x16x32_bf16 v[172:175], v[80:83], v[128:131], v[172:175]\n" \
  "s_waitcnt lgkmcnt(2)\n" \
  "v_mfma_f32_16x16x32_bf16 v[176:179], v[52:55], v[124:127], v[176:179]\n" \
  "s_setprio 1\n" \
  "ds_read_b128 v[68:71], v148 offset:12288\n" \
  "v_mfma_f32_16x16x32_bf16 v[180:183], v[52:55], v[128:131], v[180:183]\n" \
  "s_add_u32 s53, s48, 4096\n" \
  "buffer_load_dwordx4 v[100:103], %9, s[40:43], s53 offen offset:0 sc1\n" \
  "v_mfma_f32_16x16x32_bf16 v[184:187], v[56:59], v[124:127], v[184:187]\n" \
  "ds_read_b128 v[72:75], v148 offset:13312\n" \
  "v_mfma_f32_16x16x32_bf16 v[188:191], v[56:59], v[128:131], v[188:191]\n" \
  "s_add_u32 s53, s48, 4096\n" \
  "buffer_load_dwordx4 v[104:107], %9, s[40:43], s53 offen offset:1024 sc1\n" \
  "s_waitcnt lgkmcnt(2)\n" \
  "v_mfma_f32_16x16x32_bf16 v[192:195], v[124:127], v[60:63], v[192:195]\n" \
  "ds_read_b128 v[76:79], v148 offset:14336\n" \
  "v_mfma_f32_16x16x32_bf16 v[196:199], v[128:131], v[60:63], v[196:199]\n" \
  "v_mfma_f32_16x16x32_bf16 v[200:203], v[124:127], v[64:67], v[200:203]\n" \
  "ds_read_b128 v[80:83], v148 offset:15360\n" \
  "v_mfma_f32_16x16x32_bf16 v[204:207], v[128:131], v[64:67], v[204:207]\n" \
  "s_waitcnt vmcnt(11) lgkmcnt(2)\n" \
  "v_mfma_f32_16x16x32_bf16 v[160:163], v[68:71], v[132:135], v[160:163]\n" \
  "ds_read_b128 v[52:55], v148 offset:16384\n" \
  "v_mfma_f32_16x16x32_bf16 v[164:167], v[68:71], v[136:139], v[164:167]\n" \
  "s_add_u32 m0, s51, 0\n" \
  "s_nop 0\n" \
  "buffer_load_dwordx4 %11, s[44:47], s49 offen sc1 lds\n" \
  "v_mfma_f32_16x16x32_bf16 v[168:171], v[72:75], v[132:135], v[168:171]\n" \
  "ds_read_b128 v[56:59], v148 offset:17408\n" \
  "v_mfma_f32_16x16x32_bf16 v[172:175], v[72:75], v[136:139], v[172:175]\n" \
  "s_waitcnt lgkmcnt(2)\n" \
  "v_mfma_f32_16x16x32_bf16 v[176:179], v[76:79], v[132:135], v[176:179]\n" \
  "s_setprio 0\n" \
  "ds_read_b128 v[60:63], v148 offset:18432\n" \
  "v_mfma_f32_16x16x32_bf16 v[180:183], v[76:79], v[136:139], v[180:183]\n" \
  "s_add_u32 s53, s48, 4096\n" \
  "buffer_load_dwordx4 v[108:111], %9, s[40:43], s53 offen offset:2048 sc1\n" \
  "v_mfma_f32_16x16x32_bf16 v[184:187], v[80:83], v[132:135], v[184:187]\n" \
  "ds_read_b128 v[64:67], v148 offset:19456\n" \
  "v_mfma_f32_16x16x32_bf16 v[188:191], v[80:83], v[136:139], v[188:191]\n" \
  "s_add_u32 s53, s48, 4096\n" \
  "buffer_load_dwordx4 v[112:115], %9, s[40:43], s53 offen offset:3072 sc1\n" \
  "s_add_u32 s48, s48, 8192\n" \
  "s_waitcnt lgkmcnt(2)\n" \
  "v_mfma_f32_16x16x32_bf16 v[192:195], v[132:135], v[52:55], v[192:195]\n" \
  "ds_read_b128 v[68:71], v148 offset:20480\n" \
  "v_mfma_f32_16x16x32_bf16 v[196:199], v[136:139], v[52:55], v[196:199]\n" \
  "v_mfma_f32_16x16x32_bf16 v[200:203], v[132:135], v[56:59], v[200:203]\n" \
  "ds_read_b128 v[72:75], v148 offset:21504\n" \
  "v_mfma_f32_16x16x32_bf16 v[204:207], v[136:139], v[56:59], v[204:207]\n" \
  "s_waitcnt vmcnt(11) lgkmcnt(2)\n" \
  "v_mfma_f32_16x16x32_bf16 v[160:163], v[60:63], v[140:143], v[160:163]\n" \
  "ds_read_b128 v[76:79], v148 offset:22528\n" \
  "v_mfma_f32_16x16x32_bf16 v[164:167], v[60:63], v[144:147], v[164:167]\n" \
  "v_mfma_f32_16x16x32_bf16 v[168:171], v[64:67], v[140:143], v[168:171]\n" \
  "ds_read_b128 v[80:83], v148 offset:23552\n" \
  "v_mfma_f32_16x16x32_bf16 v[172:175], v[64:67], v[144:147], v[172:175]\n" \
  "s_waitcnt lgkmcnt(2)\n" \
  "v_mfma_f32_16x16x32_bf16 v[176:179], v[68:71], v[140:143], v[176:179]\n" \
  "s_add_u32 s50, s50, 24576\n" \
  "s_cmp_eq_u32 s50, 98304\n" \
  "s_cselect_b32 s50, 0, s50\n" \
  "v_add_u32 v148, s50, %10\n" \
  "s_waitcnt vmcnt(5)\n" \
  "s_barrier\n" \
  "s_setprio 3\n" \
  "ds_read_b128 v[52:55], v148 offset:0\n" \
  "v_mfma_f32_16x16x32_bf16 v[180:183], v[68:71], v[144:147], v[180:183]\n" \
  "s_add_u32 m0, s51, 8192\n" \
  "s_nop 0\n" \
  "buffer_load_dwordx4 %12, s[44:47], s49 offen sc1 lds\n" \
  "buffer_load_dwordx4 v[116:119], %9, s[40:43], s48 offen offset:0 sc1\n" \
  "v_mfma_f32_16x16x32_bf16 v[184:187], v[72:75], v[140:143], v[184:187]\n" \
  "ds_read_b128 v[56:59], v148 offset:1024\n" \
  "v_mfma_f32_16x16x32_bf16 v[188:191], v[72:75], v[144:147], v[188:191]\n" \
  "buffer_load_dwordx4 v[120:123], %9, s[40:43], s48 offen offset:1024 sc1\n" \
  "s_waitcnt lgkmcnt(2)\n" \
  "v_mfma_f32_16x16x32_bf16 v[192:195], v[140:143], v[76:79], v[192:195]\n" \
  "ds_read_b128 v[60:63], v148 offset:2048\n" \
  "v_mfma_f32_16x16x32_bf16 v[196:199], v[144:147], v[76:79], v[196:199]\n" \
  "v_mfma_f32_16x16x32_bf16 v[200:203], v[140:143], v[80:83], v[200:203]\n" \
  "ds_read_b128 v[64:67], v148 offset:3072\n" \
  "v_mfma_f32_16x16x32_bf16 v[204:207], v[144:147], v[80:83], v[204:207]\n" \
  "s_sub_u32 s52, s52, 1\n" \
  "s_cmp_lg_u32 s52, 0\n" \
  "s_cbranch_scc1 Lloop_%=\n" \
  "s_waitcnt vmcnt(11) lgkmcnt(2)\n" \
  "v_mfma_f32_16x16x32_bf16 v[160:163], v[52:55], v[84:87], v[160:163]\n" \
  "ds_read_b128 v[68:71], v148 offset:4096\n" \
  "v_mfma_f32_16x16x32_bf16 v[164:167], v[52:55], v[88:91], v[164:167]\n" \
  "v_mfma_f32_16x16x32_bf16 v[168:171], v[56:59], v[84:87], v[168:171]\n" \
  "ds_read_b128 v[72:75], v148 offset:5120\n" \
  "v_mfma_f32_16x16x32_bf16 v[172:175], v[56:59], v[88:91], v[172:175]\n" \
  "s_waitcnt lgkmcnt(2)\n" \
  "v_mfma_f32_16x16x32_bf16 v[176:179], v[60:63], v[84:87], v[176:179]\n" \
  "s_setprio 2\n" \
  "ds_read_b128 v[76:79], v148 offset:6144\n" \
  "v_mfma_f32_16x16x32_bf16 v[180:183], v[60:63], v[88:91], v[180:183]\n" \
  "s_mov_b32 s53, 57344\n" \
  "buffer_load_dwordx4 v[124:127], %9, s[40:43], s53 offen offset:2048 sc1\n" \
  "v_mfma_f32_16x16x32_bf16 v[184:187], v[64:67], v[84:87], v[184:187]\n" \
  "ds_read_b128 v[80:83], v148 offset:7168\n" \
  "v_mfma_f32_16x16x32_bf16 v[188:191], v[64:67], v[88:91], v[188:191]\n" \
  "s_mov_b32 s53, 57344\n" \
  "buffer_load_dwordx4 v[128:131], %9, s[40:43], s53 offen offset:3072 sc1\n" \
  "s_waitcnt lgkmcnt(2)\n" \
  "v_mfma_f32_16x16x32_bf16 v[192:195], v[84:87], v[68:71], v[192:195]\n" \
  "ds_read_b128 v[52:55], v148 offset:8192\n" \
  "v_mfma_f32_16x16x32_bf16 v[196:199], v[88:91], v[68:71], v[196:199]\n" \
  "s_add_u32 m0, %7, 90112\n" \
  "s_mov_b32 s53, 172032\n" \
  "buffer_load_dwordx4 %13, s[44:47], s53 offen sc1 lds\n" \
  "v_mfma_f32_16x16x32_bf16 v[200:203], v[84:87], v[72:75], v[200:203]\n" \
  "ds_read_b128 v[56:59], v148 offset:9216\n" \
  "v_mfma_f32_16x16x32_bf16 v[204:207], v[88:91], v[72:75], v[204:207]\n" \
  "s_waitcnt vmcnt(12) lgkmcnt(2)\n" \
  "v_mfma_f32_16x16x32_bf16 v[160:163], v[76:79], v[92:95], v[160:163]\n" \
  "ds_read_b128 v[60:63], v148 offset:10240\n" \
  "v_mfma_f32_16x16x32_bf16 v[164:167], v[76:79], v[96:99], v[164:167]\n" \
  "v_mfma_f32_16x16x32_bf16 v[168:171], v[80:83], v[92:95], v[168:171]\n" \
  "ds_read_b128 v[64:67], v148 offset:11264\n" \
  "v_mfma_f32_16x16x32_bf16 v[172:175], v[80:83], v[96:99], v[172:175]\n" \
  "s_waitcnt lgkmcnt(2)\n" \
  "v_mfma_f32_16x16x32_bf16 v[176:179], v[52:55], v[92:95], v[176:179]\n" \
  "s_setprio 1\n" \
  "ds_read_b128 v[68:71], v148 offset:12288\n" \
  "v_mfma_f32_16x16x32_bf16 v[180:183], v[52:55], v[96:99], v[180:183]\n" \
  "s_mov_b32 s53, 61440\n" \
  "buffer_load_dwordx4 v[132:135], %9, s[40:43], s53 offen offset:0 sc1\n" \
  "v_mfma_f32_16x16x32_bf16 v[184:187], v[56:59], v[92:95], v[184:187]\n" \
  "ds_read_b128 v[72:75], v148 offset:13312\n" \
  "v_mfma_f32_16x16x32_bf16 v[188:191], v[56:59], v[96:99], v[188:191]\n" \
  "s_mov_b32 s53, 61440\n" \
  "buffer_load_dwordx4 v[136:139], %9, s[40:43], s53 offen offset:1024 sc1\n" \
  "s_waitcnt lgkmcnt(2)\n" \
  "v_mfma_f32_16x16x32_bf16 v[192:195], v[92:95], v[60:63], v[192:195]\n" \
  "ds_read_b128 v[76:79], v148 offset:14336\n" \
  "v_mfma_f32_16x16x32_bf16 v[196:199], v[96:99], v[60:63], v[196:199]\n" \
  "v_mfma_f32_16x16x32_bf16 v[200:203], v[92:95], v[64:67], v[200:203]\n" \
  "ds_read_b128 v[80:83], v148 offset:15360\n" \
  "v_mfma_f32_16x16x32_bf16 v[204:207], v[96:99], v[64:67], v[204:207]\n" \
  "s_waitcnt vmcnt(11) lgkmcnt(2)\n" \
  "v_mfma_f32_16x16x32_bf16 v[160:163], v[68:71], v[100:103], v[160:163]\n" \
  "ds_read_b128 v[52:55], v148 offset:16384\n" \
  "v_mfma_f32_16x16x32_bf16 v[164:167], v[68:71], v[104:107], v[164:167]\n" \
  "v_mfma_f32_16x16x32_bf16 v[168:171], v[72:75], v[100:103], v[168:171]\n" \
  "ds_read_b128 v[56:59], v148 offset:17408\n" \
  "v_mfma_f32_16x16x32_bf16 v[172:175], v[72:75], v[104:107], v[172:175]\n" \
  "s_waitcnt lgkmcnt(2)\n" \
  "v_mfma_f32_16x16x32_bf16 v[176:179], v[76:79], v[100:103], v[176:179]\n" \
  "s_setprio 0\n" \
  "ds_read_b128 v[60:63], v148 offset:18432\n" \
  "v_mfma_f32_16x16x32_bf16 v[180:183], v[76:79], v[104:107], v[180:183]\n" \
  "s_mov_b32 s53, 61440\n" \
  "buffer_load_dwordx4 v[140:143], %9, s[40:43], s53 offen offset:2048 sc1\n" \
  "v_mfma_f32_16x16x32_bf16 v[184:187], v[80:83], v[100:103], v[184:187]\n" \
  "ds_read_b128 v[64:67], v148 offset:19456\n" \
  "v_mfma_f32_16x16x32_bf16 v[188:191], v[80:83], v[104:107], v[188:191]\n" \
  "s_mov_b32 s53, 61440\n" \
  "buffer_load_dwordx4 v[144:147], %9, s[40:43], s53 offen offset:3072 sc1\n" \
  "s_waitcnt lgkmcnt(2)\n" \
  "v_mfma_f32_16x16x32_bf16 v[192:195], v[100:103], v[52:55], v[192:195]\n" \
  "ds_read_b128 v[68:71], v148 offset:20480\n" \
  "v_mfma_f32_16x16x32_bf16 v[196:199], v[104:107], v[52:55], v[196:199]\n" \
  "v_mfma_f32_16x16x32_bf16 v[200:203], v[100:103], v[56:59], v[200:203]\n" \
  "ds_read_b128 v[72:75], v148 offset:21504\n" \
  "v_mfma_f32_16x16x32_bf16 v[204:207], v[104:107], v[56:59], v[204:207]\n" \
  "s_waitcnt vmcnt(10) lgkmcnt(2)\n" \
  "v_mfma_f32_16x16x32_bf16 v[160:163], v[60:63], v[108:111], v[160:163]\n" \
  "ds_read_b128 v[76:79], v148 offset:22528\n" \
  "v_mfma_f32_16x16x32_bf16 v[164:167], v[60:63], v[112:115], v[164:167]\n" \
  "v_mfma_f32_16x16x32_bf16 v[168:171], v[64:67], v[108:111], v[168:171]\n" \
  "ds_read_b128 v[80:83], v148 offset:23552\n" \
  "v_mfma_f32_16x16x32_bf16 v[172:175], v[64:67], v[112:115], v[172:175]\n" \
  "s_waitcnt lgkmcnt(2)\n" \
  "v_mfma_f32_16x16x32_bf16 v[176:179], v[68:71], v[108:111], v[176:179]\n" \
  "v_add_u32 v148, 73728, %10\n" \
  "s_waitcnt vmcnt(4)\n" \
  "s_barrier\n" \
  "ds_read_b128 v[52:55], v148 offset:0\n" \
  "v_mfma_f32_16x16x32_bf16 v[180:183], v[68:71], v[112:115], v[180:183]\n" \
  "s_add_u32 m0, %16, 0\n" \
  "s_mov_b32 s53, 0\n" \
  "buffer_load_dwordx4 %14, %15, s53 offen lds\n" \
  "v_mfma_f32_16x16x32_bf16 v[184:187], v[72:75], v[108:111], v[184:187]\n" \
  "ds_read_b128 v[56:59], v148 offset:1024\n" \
  "v_mfma_f32_16x16x32_bf16 v[188:191], v[72:75], v[112:115], v[188:191]\n" \
  "s_add_u32 m0, %16, 8448\n" \
  "s_mov_b32 s53, 131072\n" \
  "buffer_load_dwordx4 %14, %15, s53 offen lds\n" \
  "s_waitcnt lgkmcnt(2)\n" \
  "v_mfma_f32_16x16x32_bf16 v[192:195], v[108:111], v[76:79], v[192:195]\n" \
  "ds_read_b128 v[60:63], v148 offset:2048\n" \
  "v_mfma_f32_16x16x32_bf16 v[196:199], v[112:115], v[76:79], v[196:199]\n" \
  "s_add_u32 m0, %16, 16896\n" \
  "s_mov_b32 s53, 262144\n" \
  "buffer_load_dwordx4 %14, %15, s53 offen lds\n" \
  "v_mfma_f32_16x16x32_bf16 v[200:203], v[108:111], v[80:83], v[200:203]\n" \
  "ds_read_b128 v[64:67], v148 offset:3072\n" \
  "v_mfma_f32_16x16x32_bf16 v[204:207], v[112:115], v[80:83], v[204:207]\n" \
  "s_add_u32 m0, %16, 25344\n" \
  "s_mov_b32 s53, 393216\n" \
  "buffer_load_dwordx4 %14, %15, s53 offen lds\n" \
  "s_waitcnt vmcnt(11) lgkmcnt(2)\n" \
  "v_mfma_f32_16x16x32_bf16 v[160:163], v[52:55], v[116:119], v[160:163]\n" \
  "ds_read_b128 v[68:71], v148 offset:4096\n" \
  "v_mfma_f32_16x16x32_bf16 v[164:167], v[52:55], v[120:123], v[164:167]\n" \
  "s_add_u32 m0, %18, 0\n" \
  "s_nop 0\n" \
  "global_load_lds_dword %17, off\n" \
  "v_mfma_f32_16x16x32_bf16 v[168:171], v[56:59], v[116:119], v[168:171]\n" \
  "ds_read_b128 v[72:75], v148 offset:5120\n" \
  "v_mfma_f32_16x16x32_bf16 v[172:175], v[56:59], v[120:123], v[172:175]\n" \
  "s_waitcnt lgkmcnt(2)\n" \
  "v_mfma_f32_16x16x32_bf16 v[176:179], v[60:63], v[116:119], v[176:179]\n" \
  "ds_read_b128 v[76:79], v148 offset:6144\n" \
  "v_mfma_f32_16x16x32_bf16 v[180:183], v[60:63], v[120:123], v[180:183]\n" \
  "v_mfma_f32_16x16x32_bf16 v[184:187], v[64:67], v[116:119], v[184:187]\n" \
  "ds_read_b128 v[80:83], v148 offset:7168\n" \
  "v_mfma_f32_16x16x32_bf16 v[188:191], v[64:67], v[120:123], v[188:191]\n" \
  "s_waitcnt lgkmcnt(2)\n" \
  "v_mfma_f32_16x16x32_bf16 v[192:195], v[116:119], v[68:71], v[192:195]\n" \
  "ds_read_b128 v[52:55], v148 offset:8192\n" \
  "v_mfma_f32_16x16x32_bf16 v[196:199], v[120:123], v[68:71], v[196:199]\n" \
  "v_mfma_f32_16x16x32_bf16 v[200:203], v[116:119], v[72:75], v[200:203]\n" \
  "ds_read_b128 v[56:59], v148 offset:9216\n" \
  "v_mfma_f32_16x16x32_bf16 v[204:207], v[120:123], v[72:75], v[204:207]\n" \
  "s_waitcnt vmcnt(10) lgkmcnt(2)\n" \
  "v_mfma_f32_16x16x32_bf16 v[160:163], v[76:79], v[124:127], v[160:163]\n" \
  "ds_read_b128 v[60:63], v148 offset:10240\n" \
  "v_mfma_f32_16x16x32_bf16 v[164:167], v[76:79], v[128:131], v[164:167]\n" \
  "v_mfma_f32_16x16x32_bf16 v[168:171], v[80:83], v[124:127], v[168:171]\n" \
  "ds_read_b128 v[64:67], v148 offset:11264\n" \
  "v_mfma_f32_16x16x32_bf16 v[172:175], v[80:83], v[128:131], v[172:175]\n" \
  "s_waitcnt lgkmcnt(2)\n" \
  "v_mfma_f32_16x16x32_bf16 v[176:179], v[52:55], v[124:127], v[176:179]\n" \
  "ds_read_b128 v[68:71], v148 offset:12288\n" \
  "v_mfma_f32_16x16x32_bf16 v[180:183], v[52:55], v[128:131], v[180:183]\n" \
  "v_mfma_f32_16x16x32_bf16 v[184:187], v[56:59], v[124:127], v[184:187]\n" \
  "ds_read_b128 v[72:75], v148 offset:13312\n" \
  "v_mfma_f32_16x16x32_bf16 v[188:191], v[56:59], v[128:131], v[188:191]\n" \
  "s_waitcnt lgkmcnt(2)\n" \
  "v_mfma_f32_16x16x32_bf16 v[192:195], v[124:127], v[60:63], v[192:195]\n" \
  "ds_read_b128 v[76:79], v148 offset:14336\n" \
  "v_mfma_f32_16x16x32_bf16 v[196:199], v[128:131], v[60:63], v[196:199]\n" \
  "v_mfma_f32_16x16x32_bf16 v[200:203], v[124:127], v[64:67], v[200:203]\n" \
  "ds_read_b128 v[80:83], v148 offset:15360\n" \
  "v_mfma_f32_16x16x32_bf16 v[204:207], v[128:131], v[64:67], v[204:207]\n" \
  "s_waitcnt vmcnt(7) lgkmcnt(2)\n" \
  "v_mfma_f32_16x16x32_bf16 v[160:163], v[68:71], v[132:135], v[160:163]\n" \
  "ds_read_b128 v[52:55], v148 offset:16384\n" \
  "v_mfma_f32_16x16x32_bf16 v[164:167], v[68:71], v[136:139], v[164:167]\n" \
  "v_mfma_f32_16x16x32_bf16 v[168:171], v[72:75], v[132:135], v[168:171]\n" \
  "ds_read_b128 v[56:59], v148 offset:17408\n" \
  "v_mfma_f32_16x16x32_bf16 v[172:175], v[72:75], v[136:139], v[172:175]\n" \
  "s_waitcnt lgkmcnt(2)\n" \
  "v_mfma_f32_16x16x32_bf16 v[176:179], v[76:79], v[132:135], v[176:179]\n" \
  "ds_read_b128 v[60:63], v148 offset:18432\n" \
  "v_mfma_f32_16x16x32_bf16 v[180:183], v[76:79], v[136:139], v[180:183]\n" \
  "v_mfma_f32_16x16x32_bf16 v[184:187], v[80:83], v[132:135], v[184:187]\n" \
  "ds_read_b128 v[64:67], v148 offset:19456\n" \
  "v_mfma_f32_16x16x32_bf16 v[188:191], v[80:83], v[136:139], v[188:191]\n" \
  "s_waitcnt lgkmcnt(2)\n" \
  "v_mfma_f32_16x16x32_bf16 v[192:195], v[132:135], v[52:55], v[192:195]\n" \
  "ds_read_b128 v[68:71], v148 offset:20480\n" \
  "v_mfma_f32_16x16x32_bf16 v[196:199], v[136:139], v[52:55], v[196:199]\n" \
  "v_mfma_f32_16x16x32_bf16 v[200:203], v[132:135], v[56:59], v[200:203]\n" \
  "ds_read_b128 v[72:75], v148 offset:21504\n" \
  "v_mfma_f32_16x16x32_bf16 v[204:207], v[136:139], v[56:59], v[204:207]\n" \
  "s_waitcnt vmcnt(5) lgkmcnt(2)\n" \
  "v_mfma_f32_16x16x32_bf16 v[160:163], v[60:63], v[140:143], v[160:163]\n" \
  "ds_read_b128 v[76:79], v148 offset:22528\n" \
  "v_mfma_f32_16x16x32_bf16 v[164:167], v[60:63], v[144:147], v[164:167]\n" \
  "v_mfma_f32_16x16x32_bf16 v[168:171], v[64:67], v[140:143], v[168:171]\n" \
  "ds_read_b128 v[80:83], v148 offset:23552\n" \
  "v_mfma_f32_16x16x32_bf16 v[172:175], v[64:67], v[144:147], v[172:175]\n" \
  "s_waitcnt lgkmcnt(2)\n" \
  "v_mfma_f32_16x16x32_bf16 v[176:179], v[68:71], v[140:143], v[176:179]\n" \
  "v_mfma_f32_16x16x32_bf16 v[180:183], v[68:71], v[144:147], v[180:183]\n" \
  "v_mfma_f32_16x16x32_bf16 v[184:187], v[72:75], v[140:143], v[184:187]\n" \
  "v_mfma_f32_16x16x32_bf16 v[188:191], v[72:75], v[144:147], v[188:191]\n" \
  "s_waitcnt lgkmcnt(0)\n" \
  "v_mfma_f32_16x16x32_bf16 v[192:195], v[140:143], v[76:79], v[192:195]\n" \
  "v_mfma_f32_16x16x32_bf16 v[196:199], v[144:147], v[76:79], v[196:199]\n" \
  "v_mfma_f32_16x16x32_bf16 v[200:203], v[140:143], v[80:83], v[200:203]\n" \
  "v_mfma_f32_16x16x32_bf16 v[204:207], v[144:147], v[80:83], v[204:207]\n" \
  "s_nop 15\n"
#define K1_CLOB "v52", "v53", "v54", "v55", "v56", "v57", "v58", "v59", "v60", "v61", "v62", "v63", "v64", "v65", "v66", "v67", "v68", "v69", "v70", "v71", "v72", "v73", "v74", "v75", "v76", "v77", "v78", "v79", "v80", "v81", "v82", "v83", "v84", "v85", "v86", "v87", "v88", "v89", "v90", "v91", "v92", "v93", "v94", "v95", "v96", "v97", "v98", "v99", "v100", "v101", "v102", "v103", "v104", "v105", "v106", "v107", "v108", "v109", "v110", "v111", "v112", "v113", "v114", "v115", "v116", "v117", "v118", "v119", "v120", "v121", "v122", "v123", "v124", "v125", "v126", "v127", "v128", "v129", "v130", "v131", "v132", "v133", "v134", "v135", "v136", "v137", "v138", "v139", "v140", "v141", "v142", "v143", "v144", "v145", "v146", "v147", "v148", "s40", "s41", "s42", "s43", "s44", "s45", "s46", "s47", "s48", "s49", "s50", "s51", "s52", "s53", "scc", "memory"
#define K1_OUT(a, x) "=&{v[160:175]}"((a)[0]), "=&{v[176:191]}"((a)[1]), "=&{v[192:207]}"((a)[2])
#define K1_LDS 98304
#define K1_LAST 73728
#define K1_SS 24576
#define K1_NBAR 7
#define K2_PRE0 \
  "s_nop 4\n" \
  "s_mov_b64 s[44:45], %0\n" \
  "s_mov_b32 s46, %1\n" \
  "s_mov_b32 s47, 0x20000\n" \
  "s_and_b32 s45, s45, 0xffff\n" \
  "s_add_u32 m0, %2, 0\n" \
  "s_mov_b32 s53, 0\n" \
  "buffer_load_dwordx4 %4, s[44:47], s53 offen lds\n" \
  "s_add_u32 m0, %2, 8448\n" \
  "s_mov_b32 s53, 0\n" \
  "buffer_load_dwordx4 %5, s[44:47], s53 offen lds\n" \
  "s_add_u32 m0, %2, 16896\n" \
  "s_mov_b32 s53, 262144\n" \
  "buffer_load_dwordx4 %4, s[44:47], s53 offen lds\n" \
  "s_add_u32 m0, %2, 25344\n" \
  "s_mov_b32 s53, 262144\n" \
  "buffer_load_dwordx4 %5, s[44:47], s53 offen lds\n"
#define K2_PRE1 \
  "s_nop 4\n" \
  "s_mov_b64 s[44:45], %0\n" \
  "s_mov_b32 s46, %1\n" \
  "s_mov_b32 s47, 0x20000\n" \
  "s_and_b32 s45, s45, 0xffff\n" \
  "s_add_u32 m0, %2, 33792\n" \
  "s_mov_b32 s53, 524288\n" \
  "buffer_load_dwordx4 %4, s[44:47], s53 offen lds\n" \
  "s_add_u32 m0, %2, 42240\n" \
  "s_mov_b32 s53, 524288\n" \
  "buffer_load_dwordx4 %5, s[44:47], s53 offen lds\n" \
  "s_add_u32 m0, %2, 50688\n" \
  "s_mov_b32 s53, 786432\n" \
  "buffer_load_dwordx4 %4, s[44:47], s53 offen lds\n" \
  "s_add_u32 m0, %2, 59136\n" \
  "s_mov_b32 s53, 786432\n" \
  "buffer_load_dwordx4 %5, s[44:47], s53 offen lds\n" \
  "s_add_u32 m0, %2, 67584\n" \
  "s_mov_b32 s53, 1048576\n" \
  "buffer_load_dwordx4 %4, s[44:47], s53 offen lds\n" \
  "s_add_u32 m0, %2, 76032\n" \
  "s_mov_b32 s53, 1048576\n" \
  "buffer_load_dwordx4 %5, s[44:47], s53 offen lds\n"
#define K2_PRE2 \
  "s_nop 4\n" \
  "s_mov_b64 s[44:45], %0\n" \
  "s_mov_b32 s46, %1\n" \
  "s_mov_b32 s47, 0x20000\n" \
  "s_and_b32 s45, s45, 0xffff\n" \
  "s_add_u32 m0, %2, 84480\n" \
  "s_mov_b32 s53, 1310720\n" \
  "buffer_load_dwordx4 %4, s[44:47], s53 offen lds\n" \
  "s_add_u32 m0, %2, 92928\n" \
  "s_mov_b32 s53, 1310720\n" \
  "buffer_load_dwordx4 %5, s[44:47], s53 offen lds\n" \
  "s_add_u32 m0, %2, 101376\n" \
  "s_mov_b32 s53, 1572864\n" \
  "buffer_load_dwordx4 %4, s[44:47], s53 offen lds\n" \
  "s_add_u32 m0, %2, 109824\n" \
  "s_mov_b32 s53, 1572864\n" \
  "buffer_load_dwordx4 %5, s[44:47], s53 offen lds\n" \
  "s_add_u32 m0, %2, 118272\n" \
  "s_mov_b32 s53, 1835008\n" \
  "buffer_load_dwordx4 %4, s[44:47], s53 offen lds\n" \
  "s_add_u32 m0, %2, 126720\n" \
  "s_mov_b32 s53, 1835008\n" \
  "buffer_load_dwordx4 %5, s[44:47], s53 offen lds\n"
#define K2_NPRE 16
#define K2_PRECLOB "s40", "s41", "s42", "s43", "s44", "s45", "s46", "s47", "s48", "s49", "s50", "s51", "s52", "s53", "scc", "memory"
#define K2_ASM \
  "s_nop 4\n" \
  "s_mov_b64 s[44:45], %3\n" \
  "s_mov_b32 s46, %4\n" \
  "s_mov_b32 s47, 0x20000\n" \
  "s_mov_b64 s[40:41], %1\n" \
  "s_mov_b32 s42, %2\n" \
  "s_mov_b32 s43, 0x20000\n" \
  "s_and_b32 s41, s41, 0xffff\n" \
  "s_and_b32 s45, s45, 0xffff\n" \
  "s_mov_b32 s53, 0\n" \
  "buffer_load_dwordx4 v[128:131], %7, s[40:43], s53 offen offset:0 sc1\n" \
  "s_mov_b32 s53, 0\n" \
  "buffer_load_dwordx4 v[132:135], %7, s[40:43], s53 offen offset:2048 sc1\n" \
  "s_mov_b32 s53, 4096\n" \
  "buffer_load_dwordx4 v[136:139], %7, s[40:43], s53 offen offset:0 sc1\n" \
  "s_mov_b32 s53, 4096\n" \
  "buffer_load_dwordx4 v[140:143], %7, s[40:43], s53 offen offset:2048 sc1\n" \
  "s_mov_b32 s53, 8192\n" \
  "buffer_load_dwordx4 v[144:147], %7, s[40:43], s53 offen offset:0 sc1\n" \
  "s_mov_b32 s53, 8192\n" \
  "buffer_load_dwordx4 v[148:151], %7, s[40:43], s53 offen offset:2048 sc1\n" \
  "s_mov_b32 s53, 12288\n" \
  "buffer_load_dwordx4 v[152:155], %7, s[40:43], s53 offen offset:0 sc1\n" \
  "v_add_u32 v192, 0, %8\n" \
  "s_waitcnt lgkmcnt(0)\n" \
  "ds_read_b64_tr_b16 v[96:97], v192 offset:0\n" \
  "ds_read_b64_tr_b16 v[98:99], v192 offset:256\n" \
  "ds_read_b64_tr_b16 v[100:101], v192 offset:32\n" \
  "ds_read_b64_tr_b16 v[102:103], v192 offset:288\n" \
  "ds_read_b64_tr_b16 v[104:105], v192 offset:64\n" \
  "ds_read_b64_tr_b16 v[106:107], v192 offset:320\n" \
  "ds_read_b64_tr_b16 v[108:109], v192 offset:96\n" \
  "ds_read_b64_tr_b16 v[110:111], v192 offset:352\n" \
  "s_waitcnt vmcnt(6) lgkmcnt(4)\n" \
  "v_mfma_f32_16x16x32_bf16 v[200:203], v[96:99], v[128:131], 0\n" \
  "ds_read_b64_tr_b16 v[112:113], v192 offset:512\n" \
  "ds_read_b64_tr_b16 v[114:115], v192 offset:768\n" \
  "s_mov_b32 s53, 12288\n" \
  "buffer_load_dwordx4 v[156:159], %7, s[40:43], s53 offen offset:2048 sc1\n" \
  "v_mfma_f32_16x16x32_bf16 v[204:207], v[100:103], v[128:131], 0\n" \
  "ds_read_b64_tr_b16 v[116:117], v192 offset:544\n" \
  "ds_read_b64_tr_b16 v[118:119], v192 offset:800\n" \
  "s_waitcnt lgkmcnt(4)\n" \
  "v_mfma_f32_16x16x32_bf16 v[208:211], v[104:107], v[128:131], 0\n" \
  "ds_read_b64_tr_b16 v[120:121], v192 offset:576\n" \
  "ds_read_b64_tr_b16 v[122:123], v192 offset:832\n" \
  "v_mfma_f32_16x16x32_bf16 v[212:215], v[108:111], v[128:131], 0\n" \
  "ds_read_b64_tr_b16 v[124:125], v192 offset:608\n" \
  "ds_read_b64_tr_b16 v[126:127], v192 offset:864\n" \
  "s_waitcnt vmcnt(6) lgkmcnt(4)\n" \
  "v_mfma_f32_16x16x32_bf16 v[200:203], v[112:115], v[132:135], v[200:203]\n" \
  "ds_read_b64_tr_b16 v[96:97], v192 offset:8448\n" \
  "ds_read_b64_tr_b16 v[98:99], v192 offset:8704\n" \
  "s_mov_b32 s53, 16384\n" \
  "buffer_load_dwordx4 v[160:163], %7, s[40:43], s53 offen offset:0 sc1\n" \
  "v_mfma_f32_16x16x32_bf16 v[204:207], v[116:119], v[132:135], v[204:207]\n" \
  "ds_read_b64_tr_b16 v[100:101], v192 offset:8480\n" \
  "ds_read_b64_tr_b16 v[102:103], v192 offset:8736\n" \
  "s_waitcnt lgkmcnt(4)\n" \
  "v_mfma_f32_16x16x32_bf16 v[208:211], v[120:123], v[132:135], v[208:211]\n" \
  "ds_read_b64_tr_b16 v[104:105], v192 offset:8512\n" \
  "ds_read_b64_tr_b16 v[106:107], v192 offset:8768\n" \
  "v_mfma_f32_16x16x32_bf16 v[212:215], v[124:127], v[132:135], v[212:215]\n" \
  "ds_read_b64_tr_b16 v[108:109], v192 offset:8544\n" \
  "ds_read_b64_tr_b16 v[110:111], v192 offset:8800\n" \
  "s_waitcnt vmcnt(6) lgkmcnt(4)\n" \
  "v_mfma_f32_16x16x32_bf16 v[200:203], v[96:99], v[136:139], v[200:203]\n" \
  "ds_read_b64_tr_b16 v[112:113], v192 offset:8960\n" \
  "ds_read_b64_tr_b16 v[114:115], v192 offset:9216\n" \
  "s_mov_b32 s53, 16384\n" \
  "buffer_load_dwordx4 v[164:167], %7, s[40:43], s53 offen offset:2048 sc1\n" \
  "v_mfma_f32_16x16x32_bf16 v[204:207], v[100:103], v[136:139], v[204:207]\n" \
  "ds_read_b64_tr_b16 v[116:117], v192 offset:8992\n" \
  "ds_read_b64_tr_b16 v[118:119], v192 offset:9248\n" \
  "s_waitcnt lgkmcnt(4)\n" \
  "v_mfma_f32_16x16x32_bf16 v[208:211], v[104:107], v[136:139], v[208:211]\n" \
  "ds_read_b64_tr_b16 v[120:121], v192 offset:9024\n" \
  "ds_read_b64_tr_b16 v[122:123], v192 offset:9280\n" \
  "v_mfma_f32_16x16x32_bf16 v[212:215], v[108:111], v[136:139], v[212:215]\n" \
  "ds_read_b64_tr_b16 v[124:125], v192 offset:9056\n" \
  "ds_read_b64_tr_b16 v[126:127], v192 offset:9312\n" \
  "s_waitcnt vmcnt(6) lgkmcnt(4)\n" \
  "v_mfma_f32_16x16x32_bf16 v[200:203], v[112:115], v[140:143], v[200:203]\n" \
  "v_add_u32 v192, 16896, %8\n" \
  "ds_read_b64_tr_b16 v[96:97], v192 offset:0\n" \
  "ds_read_b64_tr_b16 v[98:99], v192 offset:256\n" \
  "s_mov_b32 s53, 20480\n" \
  "buffer_load_dwordx4 v[168:171], %7, s[40:43], s53 offen offset:0 sc1\n" \
  "v_mfma_f32_16x16x32_bf16 v[204:207], v[116:119], v[140:143], v[204:207]\n" \
  "ds_read_b64_tr_b16 v[100:101], v192 offset:32\n" \
  "ds_read_b64_tr_b16 v[102:103], v192 offset:288\n" \
  "s_waitcnt lgkmcnt(4)\n" \
  "v_mfma_f32_16x16x32_bf16 v[208:211], v[120:123], v[140:143], v[208:211]\n" \
  "ds_read_b64_tr_b16 v[104:105], v192 offset:64\n" \
  "ds_read_b64_tr_b16 v[106:107], v192 offset:320\n" \
  "v_mfma_f32_16x16x32_bf16 v[212:215], v[124:127], v[140:143], v[212:215]\n" \
  "ds_read_b64_tr_b16 v[108:109], v192 offset:96\n" \
  "ds_read_b64_tr_b16 v[110:111], v192 offset:352\n" \
  "s_waitcnt vmcnt(6) lgkmcnt(4)\n" \
  "v_mfma_f32_16x16x32_bf16 v[200:203], v[96:99], v[144:147], v[200:203]\n" \
  "ds_read_b64_tr_b16 v[112:113], v192 offset:512\n" \
  "ds_read_b64_tr_b16 v[114:115], v192 offset:768\n" \
  "s_mov_b32 s53, 20480\n" \
  "buffer_load_dwordx4 v[172:175], %7, s[40:43], s53 offen offset:2048 sc1\n" \
  "v_mfma_f32_16x16x32_bf16 v[204:207], v[100:103], v[144:147], v[204:207]\n" \
  "ds_read_b64_tr_b16 v[116:117], v192 offset:544\n" \
  "ds_read_b64_tr_b16 v[118:119], v192 offset:800\n" \
  "s_waitcnt lgkmcnt(4)\n" \
  "v_mfma_f32_16x16x32_bf16 v[208:211], v[104:107], v[144:147], v[208:211]\n" \
  "ds_read_b64_tr_b16 v[120:121], v192 offset:576\n" \
  "ds_read_b64_tr_b16 v[122:123], v192 offset:832\n" \
  "v_mfma_f32_16x16x32_bf16 v[212:215], v[108:111], v[144:147], v[212:215]\n" \
  "ds_read_b64_tr_b16 v[124:125], v192 offset:608\n" \
  "ds_read_b64_tr_b16 v[126:127], v192 offset:864\n" \
  "s_waitcnt vmcnt(6) lgkmcnt(4)\n" \
  "v_mfma_f32_16x16x32_bf16 v[200:203], v[112:115], v[148:151], v[200:203]\n" \
  "ds_read_b64_tr_b16 v[96:97], v192 offset:8448\n" \
  "ds_read_b64_tr_b16 v[98:99], v192 offset:8704\n" \
  "s_mov_b32 s53, 24576\n" \
  "buffer_load_dwordx4 v[176:179], %7, s[40:43], s53 offen offset:0 sc1\n" \
  "v_mfma_f32_16x16x32_bf16 v[204:207], v[116:119], v[148:151], v[204:207]\n" \
  "ds_read_b64_tr_b16 v[100:101], v192 offset:8480\n" \
  "ds_read_b64_tr_b16 v[102:103], v192 offset:8736\n" \
  "s_waitcnt lgkmcnt(4)\n" \
  "v_mfma_f32_16x16x32_bf16 v[208:211], v[120:123], v[148:151], v[208:211]\n" \
  "ds_read_b64_tr_b16 v[104:105], v192 offset:8512\n" \
  "ds_read_b64_tr_b16 v[106:107], v192 offset:8768\n" \
  "v_mfma_f32_16x16x32_bf16 v[212:215], v[124:127], v[148:151], v[212:215]\n" \
  "ds_read_b64_tr_b16 v[108:109], v192 offset:8544\n" \
  "ds_read_b64_tr_b16 v[110:111], v192 offset:8800\n" \
  "s_waitcnt vmcnt(6) lgkmcnt(4)\n" \
  "v_mfma_f32_16x16x32_bf16 v[200:203], v[96:99], v[152:155], v[200:203]\n" \
  "ds_read_b64_tr_b16 v[112:113], v192 offset:8960\n" \
  "ds_read_b64_tr_b16 v[114:115], v192 offset:9216\n" \
  "s_mov_b32 s53, 24576\n" \
  "buffer_load_dwordx4 v[180:183], %7, s[40:43], s53 offen offset:2048 sc1\n" \
  "v_mfma_f32_16x16x32_bf16 v[204:207], v[100:103], v[152:155], v[204:207]\n" \
  "ds_read_b64_tr_b16 v[116:117], v192 offset:8992\n" \
  "ds_read_b64_tr_b16 v[118:119], v192 offset:9248\n" \
  "s_waitcnt lgkmcnt(4)\n" \
  "v_mfma_f32_16x16x32_bf16 v[208:211], v[104:107], v[152:155], v[208:211]\n" \
  "ds_read_b64_tr_b16 v[120:121], v192 offset:9024\n" \
  "ds_read_b64_tr_b16 v[122:123], v192 offset:9280\n" \
  "v_mfma_f32_16x16x32_bf16 v[212:215], v[108:111], v[152:155], v[212:215]\n" \
  "ds_read_b64_tr_b16 v[124:125], v192 offset:9056\n" \
  "ds_read_b64_tr_b16 v[126:127], v192 offset:9312\n" \
  "s_waitcnt vmcnt(6) lgkmcnt(4)\n" \
  "v_mfma_f32_16x16x32_bf16 v[200:203], v[112:115], v[156:159], v[200:203]\n" \
  "v_add_u32 v192, 33792, %8\n" \
  "ds_read_b64_tr_b16 v[96:97], v192 offset:0\n" \
  "ds_read_b64_tr_b16 v[98:99], v192 offset:256\n" \
  "s_mov_b32 s53, 28672\n" \
  "buffer_load_dwordx4 v[184:187], %7, s[40:43], s53 offen offset:0 sc1\n" \
  "v_mfma_f32_16x16x32_bf16 v[204:207], v[116:119], v[156:159], v[204:207]\n" \
  "ds_read_b64_tr_b16 v[100:101], v192 offset:32\n" \
  "ds_read_b64_tr_b16 v[102:103], v192 offset:288\n" \
  "s_waitcnt lgkmcnt(4)\n" \
  "v_mfma_f32_16x16x32_bf16 v[208:211], v[120:123], v[156:159], v[208:211]\n" \
  "ds_read_b64_tr_b16 v[104:105], v192 offset:64\n" \
  "ds_read_b64_tr_b16 v[106:107], v192 offset:320\n" \
  "v_mfma_f32_16x16x32_bf16 v[212:215], v[124:127], v[156:159], v[212:215]\n" \
  "ds_read_b64_tr_b16 v[108:109], v192 offset:96\n" \
  "ds_read_b64_tr_b16 v[110:111], v192 offset:352\n" \
  "s_waitcnt vmcnt(6) lgkmcnt(4)\n" \
  "v_mfma_f32_16x16x32_bf16 v[200:203], v[96:99], v[160:163], v[200:203]\n" \
  "ds_read_b64_tr_b16 v[112:113], v192 offset:512\n" \
  "ds_read_b64_tr_b16 v[114:115], v192 offset:768\n" \
  "s_mov_b32 s53, 28672\n" \
  "buffer_load_dwordx4 v[188:191], %7, s[40:43], s53 offen offset:2048 sc1\n" \
  "v_mfma_f32_16x16x32_bf16 v[204:207], v[100:103], v[160:163], v[204:207]\n" \
  "ds_read_b64_tr_b16 v[116:117], v192 offset:544\n" \
  "ds_read_b64_tr_b16 v[118:119], v192 offset:800\n" \
  "s_waitcnt lgkmcnt(4)\n" \
  "v_mfma_f32_16x16x32_bf16 v[208:211], v[104:107], v[160:163], v[208:211]\n" \
  "ds_read_b64_tr_b16 v[120:121], v192 offset:576\n" \
  "ds_read_b64_tr_b16 v[122:123], v192 offset:832\n" \
  "v_mfma_f32_16x16x32_bf16 v[212:215], v[108:111], v[160:163], v[212:215]\n" \
  "ds_read_b64_tr_b16 v[124:125], v192 offset:608\n" \
  "ds_read_b64_tr_b16 v[126:127], v192 offset:864\n" \
  "s_waitcnt vmcnt(6) lgkmcnt(4)\n" \
  "v_mfma_f32_16x16x32_bf16 v[200:203], v[112:115], v[164:167], v[200:203]\n" \
  "ds_read_b64_tr_b16 v[96:97], v192 offset:8448\n" \
  "ds_read_b64_tr_b16 v[98:99], v192 offset:8704\n" \
  "s_mov_b32 s53, 32768\n" \
  "buffer_load_dwordx4 v[128:131], %7, s[40:43], s53 offen offset:0 sc1\n" \
  "v_mfma_f32_16x16x32_bf16 v[204:207], v[116:119], v[164:167], v[204:207]\n" \
  "ds_read_b64_tr_b16 v[100:101], v192 offset:8480\n" \
  "ds_read_b64_tr_b16 v[102:103], v192 offset:8736\n" \
  "s_waitcnt lgkmcnt(4)\n" \
  "v_mfma_f32_16x16x32_bf16 v[208:211], v[120:123], v[164:167], v[208:211]\n" \
  "ds_read_b64_tr_b16 v[104:105], v192 offset:8512\n" \
  "ds_read_b64_tr_b16 v[106:107], v192 offset:8768\n" \
  "v_mfma_f32_16x16x32_bf16 v[212:215], v[124:127], v[164:167], v[212:215]\n" \
  "ds_read_b64_tr_b16 v[108:109], v192 offset:8544\n" \
  "ds_read_b64_tr_b16 v[110:111], v192 offset:8800\n" \
  "s_waitcnt vmcnt(6) lgkmcnt(4)\n" \
  "v_mfma_f32_16x16x32_bf16 v[200:203], v[96:99], v[168:171], v[200:203]\n" \
  "ds_read_b64_tr_b16 v[112:113], v192 offset:8960\n" \
  "ds_read_b64_tr_b16 v[114:115], v192 offset:9216\n" \
  "s_mov_b32 s53, 32768\n" \
  "buffer_load_dwordx4 v[132:135], %7, s[40:43], s53 offen offset:2048 sc1\n" \
  "v_mfma_f32_16x16x32_bf16 v[204:207], v[100:103], v[168:171], v[204:207]\n" \
  "ds_read_b64_tr_b16 v[116:117], v192 offset:8992\n" \
  "ds_read_b64_tr_b16 v[118:119], v192 offset:9248\n" \
  "s_waitcnt lgkmcnt(4)\n" \
  "v_mfma_f32_16x16x32_bf16 v[208:211], v[104:107], v[168:171], v[208:211]\n" \
  "ds_read_b64_tr_b16 v[120:121], v192 offset:9024\n" \
  "ds_read_b64_tr_b16 v[122:123], v192 offset:9280\n" \
  "v_mfma_f32_16x16x32_bf16 v[212:215], v[108:111], v[168:171], v[212:215]\n" \
  "ds_read_b64_tr_b16 v[124:125], v192 offset:9056\n" \
  "ds_read_b64_tr_b16 v[126:127], v192 offset:9312\n" \
  "s_waitcnt vmcnt(6) lgkmcnt(4)\n" \
  "v_mfma_f32_16x16x32_bf16 v[200:203], v[112:115], v[172:175], v[200:203]\n" \
  "v_add_u32 v192, 50688, %8\n" \
  "ds_read_b64_tr_b16 v[96:97], v192 offset:0\n" \
  "ds_read_b64_tr_b16 v[98:99], v192 offset:256\n" \
  "s_mov_b32 s53, 36864\n" \
  "buffer_load_dwordx4 v[136:139], %7, s[40:43], s53 offen offset:0 sc1\n" \
  "v_mfma_f32_16x16x32_bf16 v[204:207], v[116:119], v[172:175], v[204:207]\n" \
  "ds_read_b64_tr_b16 v[100:101], v192 offset:32\n" \
  "ds_read_b64_tr_b16 v[102:103], v192 offset:288\n" \
  "s_waitcnt lgkmcnt(4)\n" \
  "v_mfma_f32_16x16x32_bf16 v[208:211], v[120:123], v[172:175], v[208:211]\n" \
  "ds_read_b64_tr_b16 v[104:105], v192 offset:64\n" \
  "ds_read_b64_tr_b16 v[106:107], v192 offset:320\n" \
  "v_mfma_f32_16x16x32_bf16 v[212:215], v[124:127], v[172:175], v[212:215]\n" \
  "ds_read_b64_tr_b16 v[108:109], v192 offset:96\n" \
  "ds_read_b64_tr_b16 v[110:111], v192 offset:352\n" \
  "s_waitcnt vmcnt(6) lgkmcnt(4)\n" \
  "v_mfma_f32_16x16x32_bf16 v[200:203], v[96:99], v[176:179], v[200:203]\n" \
  "ds_read_b64_tr_b16 v[112:113], v192 offset:512\n" \
  "ds_read_b64_tr_b16 v[114:115], v192 offset:768\n" \
  "s_mov_b32 s53, 36864\n" \
  "buffer_load_dwordx4 v[140:143], %7, s[40:43], s53 offen offset:2048 sc1\n" \
  "v_mfma_f32_16x16x32_bf16 v[204:207], v[100:103], v[176:179], v[204:207]\n" \
  "ds_read_b64_tr_b16 v[116:117], v192 offset:544\n" \
  "ds_read_b64_tr_b16 v[118:119], v192 offset:800\n" \
  "s_waitcnt lgkmcnt(4)\n" \
  "v_mfma_f32_16x16x32_bf16 v[208:211], v[104:107], v[176:179], v[208:211]\n" \
  "ds_read_b64_tr_b16 v[120:121], v192 offset:576\n" \
  "ds_read_b64_tr_b16 v[122:123], v192 offset:832\n" \
  "v_mfma_f32_16x16x32_bf16 v[212:215], v[108:111], v[176:179], v[212:215]\n" \
  "ds_read_b64_tr_b16 v[124:125], v192 offset:608\n" \
  "ds_read_b64_tr_b16 v[126:127], v192 offset:864\n" \
  "s_waitcnt vmcnt(6) lgkmcnt(4)\n" \
  "v_mfma_f32_16x16x32_bf16 v[200:203], v[112:115], v[180:183], v[200:203]\n" \
  "ds_read_b64_tr_b16 v[96:97], v192 offset:8448\n" \
  "ds_read_b64_tr_b16 v[98:99], v192 offset:8704\n" \
  "s_mov_b32 s53, 40960\n" \
  "buffer_load_dwordx4 v[144:147], %7, s[40:43], s53 offen offset:0 sc1\n" \
  "v_mfma_f32_16x16x32_bf16 v[204:207], v[116:119], v[180:183], v[204:207]\n" \
  "ds_read_b64_tr_b16 v[100:101], v192 offset:8480\n" \
  "ds_read_b64_tr_b16 v[102:103], v192 offset:8736\n" \
  "s_waitcnt lgkmcnt(4)\n" \
  "v_mfma_f32_16x16x32_bf16 v[208:211], v[120:123], v[180:183], v[208:211]\n" \
  "ds_read_b64_tr_b16 v[104:105], v192 offset:8512\n" \
  "ds_read_b64_tr_b16 v[106:107], v192 offset:8768\n" \
  "v_mfma_f32_16x16x32_bf16 v[212:215], v[124:127], v[180:183], v[212:215]\n" \
  "ds_read_b64_tr_b16 v[108:109], v192 offset:8544\n" \
  "ds_read_b64_tr_b16 v[110:111], v192 offset:8800\n" \
  "s_waitcnt vmcnt(6) lgkmcnt(4)\n" \
  "v_mfma_f32_16x16x32_bf16 v[200:203], v[96:99], v[184:187], v[200:203]\n" \
  "ds_read_b64_tr_b16 v[112:113], v192 offset:8960\n" \
  "ds_read_b64_tr_b16 v[114:115], v192 offset:9216\n" \
  "s_mov_b32 s53, 40960\n" \
  "buffer_load_dwordx4 v[148:151], %7, s[40:43], s53 offen offset:2048 sc1\n" \
  "v_mfma_f32_16x16x32_bf16 v[204:207], v[100:103], v[184:187], v[204:207]\n" \
  "ds_read_b64_tr_b16 v[116:117], v192 offset:8992\n" \
  "ds_read_b64_tr_b16 v[118:119], v192 offset:9248\n" \
  "s_waitcnt lgkmcnt(4)\n" \
  "v_mfma_f32_16x16x32_bf16 v[208:211], v[104:107], v[184:187], v[208:211]\n" \
  "ds_read_b64_tr_b16 v[120:121], v192 offset:9024\n" \
  "ds_read_b64_tr_b16 v[122:123], v192 offset:9280\n" \
  "v_mfma_f32_16x16x32_bf16 v[212:215], v[108:111], v[184:187], v[212:215]\n" \
  "ds_read_b64_tr_b16 v[124:125], v192 offset:9056\n" \
  "ds_read_b64_tr_b16 v[126:127], v192 offset:9312\n" \
  "s_waitcnt vmcnt(6) lgkmcnt(4)\n" \
  "v_mfma_f32_16x16x32_bf16 v[200:203], v[112:115], v[188:191], v[200:203]\n" \
  "v_add_u32 v192, 67584, %8\n" \
  "ds_read_b64_tr_b16 v[96:97], v192 offset:0\n" \
  "ds_read_b64_tr_b16 v[98:99], v192 offset:256\n" \
  "s_mov_b32 s53, 45056\n" \
  "buffer_load_dwordx4 v[152:155], %7, s[40:43], s53 offen offset:0 sc1\n" \
  "v_mfma_f32_16x16x32_bf16 v[204:207], v[116:119], v[188:191], v[204:207]\n" \
  "ds_read_b64_tr_b16 v[100:101], v192 offset:32\n" \
  "ds_read_b64_tr_b16 v[102:103], v192 offset:288\n" \
  "s_waitcnt lgkmcnt(4)\n" \
  "v_mfma_f32_16x16x32_bf16 v[208:211], v[120:123], v[188:191], v[208:211]\n" \
  "ds_read_b64_tr_b16 v[104:105], v192 offset:64\n" \
  "ds_read_b64_tr_b16 v[106:107], v192 offset:320\n" \
  "v_mfma_f32_16x16x32_bf16 v[212:215], v[124:127], v[188:191], v[212:215]\n" \
  "ds_read_b64_tr_b16 v[108:109], v192 offset:96\n" \
  "ds_read_b64_tr_b16 v[110:111], v192 offset:352\n" \
  "s_waitcnt vmcnt(6) lgkmcnt(4)\n" \
  "v_mfma_f32_16x16x32_bf16 v[200:203], v[96:99], v[128:131], v[200:203]\n" \
  "ds_read_b64_tr_b16 v[112:113], v192 offset:512\n" \
  "ds_read_b64_tr_b16 v[114:115], v192 offset:768\n" \
  "s_mov_b32 s53, 45056\n" \
  "buffer_load_dwordx4 v[156:159], %7, s[40:43], s53 offen offset:2048 sc1\n" \
  "v_mfma_f32_16x16x32_bf16 v[204:207], v[100:103], v[128:131], v[204:207]\n" \
  "ds_read_b64_tr_b16 v[116:117], v192 offset:544\n" \
  "ds_read_b64_tr_b16 v[118:119], v192 offset:800\n" \
  "s_waitcnt lgkmcnt(4)\n" \
  "v_mfma_f32_16x16x32_bf16 v[208:211], v[104:107], v[128:131], v[208:211]\n" \
  "ds_read_b64_tr_b16 v[120:121], v192 offset:576\n" \
  "ds_read_b64_tr_b16 v[122:123], v192 offset:832\n" \
  "v_mfma_f32_16x16x32_bf16 v[212:215], v[108:111], v[128:131], v[212:215]\n" \
  "ds_read_b64_tr_b16 v[124:125], v192 offset:608\n" \
  "ds_read_b64_tr_b16 v[126:127], v192 offset:864\n" \
  "s_waitcnt vmcnt(6) lgkmcnt(4)\n" \
  "v_mfma_f32_16x16x32_bf16 v[200:203], v[112:115], v[132:135], v[200:203]\n" \
  "ds_read_b64_tr_b16 v[96:97], v192 offset:8448\n" \
  "ds_read_b64_tr_b16 v[98:99], v192 offset:8704\n" \
  "s_mov_b32 s53, 49152\n" \
  "buffer_load_dwordx4 v[160:163], %7, s[40:43], s53 offen offset:0 sc1\n" \
  "v_mfma_f32_16x16x32_bf16 v[204:207], v[116:119], v[132:135], v[204:207]\n" \
  "ds_read_b64_tr_b16 v[100:101], v192 offset:8480\n" \
  "ds_read_b64_tr_b16 v[102:103], v192 offset:8736\n" \
  "s_waitcnt lgkmcnt(4)\n" \
  "v_mfma_f32_16x16x32_bf16 v[208:211], v[120:123], v[132:135], v[208:211]\n" \
  "ds_read_b64_tr_b16 v[104:105], v192 offset:8512\n" \
  "ds_read_b64_tr_b16 v[106:107], v192 offset:8768\n" \
  "v_mfma_f32_16x16x32_bf16 v[212:215], v[124:127], v[132:135], v[212:215]\n" \
  "ds_read_b64_tr_b16 v[108:109], v192 offset:8544\n" \
  "ds_read_b64_tr_b16 v[110:111], v192 offset:8800\n" \
  "s_waitcnt vmcnt(6) lgkmcnt(4)\n" \
  "v_mfma_f32_16x16x32_bf16 v[200:203], v[96:99], v[136:139], v[200:203]\n" \
  "ds_read_b64_tr_b16 v[112:113], v192 offset:8960\n" \
  "ds_read_b64_tr_b16 v[114:115], v192 offset:9216\n" \
  "s_mov_b32 s53, 49152\n" \
  "buffer_load_dwordx4 v[164:167], %7, s[40:43], s53 offen offset:2048 sc1\n" \
  "v_mfma_f32_16x16x32_bf16 v[204:207], v[100:103], v[136:139], v[204:207]\n" \
  "ds_read_b64_tr_b16 v[116:117], v192 offset:8992\n" \
  "ds_read_b64_tr_b16 v[118:119], v192 offset:9248\n" \
  "s_waitcnt lgkmcnt(4)\n" \
  "v_mfma_f32_16x16x32_bf16 v[208:211], v[104:107], v[136:139], v[208:211]\n" \
  "ds_read_b64_tr_b16 v[120:121], v192 offset:9024\n" \
  "ds_read_b64_tr_b16 v[122:123], v192 offset:9280\n" \
  "v_mfma_f32_16x16x32_bf16 v[212:215], v[108:111], v[136:139], v[212:215]\n" \
  "ds_read_b64_tr_b16 v[124:125], v192 offset:9056\n" \
  "ds_read_b64_tr_b16 v[126:127], v192 offset:9312\n" \
  "s_waitcnt vmcnt(6) lgkmcnt(4)\n" \
  "v_mfma_f32_16x16x32_bf16 v[200:203], v[112:115], v[140:143], v[200:203]\n" \
  "v_add_u32 v192, 84480, %8\n" \
  "ds_read_b64_tr_b16 v[96:97], v192 offset:0\n" \
  "ds_read_b64_tr_b16 v[98:99], v192 offset:256\n" \
  "s_mov_b32 s53, 53248\n" \
  "buffer_load_dwordx4 v[168:171], %7, s[40:43], s53 offen offset:0 sc1\n" \
  "v_mfma_f32_16x16x32_bf16 v[204:207], v[116:119], v[140:143], v[204:207]\n" \
  "ds_read_b64_tr_b16 v[100:101], v192 offset:32\n" \
  "ds_read_b64_tr_b16 v[102:103], v192 offset:288\n" \
  "s_waitcnt lgkmcnt(4)\n" \
  "v_mfma_f32_16x16x32_bf16 v[208:211], v[120:123], v[140:143], v[208:211]\n" \
  "ds_read_b64_tr_b16 v[104:105], v192 offset:64\n" \
  "ds_read_b64_tr_b16 v[106:107], v192 offset:320\n" \
  "v_mfma_f32_16x16x32_bf16 v[212:215], v[124:127], v[140:143], v[212:215]\n" \
  "ds_read_b64_tr_b16 v[108:109], v192 offset:96\n" \
  "ds_read_b64_tr_b16 v[110:111], v192 offset:352\n" \
  "s_waitcnt vmcnt(6) lgkmcnt(4)\n" \
  "v_mfma_f32_16x16x32_bf16 v[200:203], v[96:99], v[144:147], v[200:203]\n" \
  "ds_read_b64_tr_b16 v[112:113], v192 offset:512\n" \
  "ds_read_b64_tr_b16 v[114:115], v192 offset:768\n" \
  "s_mov_b32 s53, 53248\n" \
  "buffer_load_dwordx4 v[172:175], %7, s[40:43], s53 offen offset:2048 sc1\n" \
  "s_add_u32 m0, %13, 0\n" \
  "s_nop 0\n" \
  "global_load_lds_dwordx4 %11, off\n" \
  "v_mfma_f32_16x16x32_bf16 v[204:207], v[100:103], v[144:147], v[204:207]\n" \
  "ds_read_b64_tr_b16 v[116:117], v192 offset:544\n" \
  "ds_read_b64_tr_b16 v[118:119], v192 offset:800\n" \
  "s_add_u32 m0, %13, 1024\n" \
  "s_nop 0\n" \
  "global_load_lds_dwordx4 %12, off\n" \
  "s_waitcnt lgkmcnt(4)\n" \
  "v_mfma_f32_16x16x32_bf16 v[208:211], v[104:107], v[144:147], v[208:211]\n" \
  "ds_read_b64_tr_b16 v[120:121], v192 offset:576\n" \
  "ds_read_b64_tr_b16 v[122:123], v192 offset:832\n" \
  "v_mfma_f32_16x16x32_bf16 v[212:215], v[108:111], v[144:147], v[212:215]\n" \
  "ds_read_b64_tr_b16 v[124:125], v192 offset:608\n" \
  "ds_read_b64_tr_b16 v[126:127], v192 offset:864\n" \
  "s_waitcnt vmcnt(8) lgkmcnt(4)\n" \
  "v_mfma_f32_16x16x32_bf16 v[200:203], v[112:115], v[148:151], v[200:203]\n" \
  "ds_read_b64_tr_b16 v[96:97], v192 offset:8448\n" \
  "ds_read_b64_tr_b16 v[98:99], v192 offset:8704\n" \
  "s_mov_b32 s53, 57344\n" \
  "buffer_load_dwordx4 v[176:179], %7, s[40:43], s53 offen offset:0 sc1\n" \
  "v_mfma_f32_16x16x32_bf16 v[204:207], v[116:119], v[148:151], v[204:207]\n" \
  "ds_read_b64_tr_b16 v[100:101], v192 offset:8480\n" \
  "ds_read_b64_tr_b16 v[102:103], v192 offset:8736\n" \
  "s_waitcnt lgkmcnt(4)\n" \
  "v_mfma_f32_16x16x32_bf16 v[208:211], v[120:123], v[148:151], v[208:211]\n" \
  "ds_read_b64_tr_b16 v[104:105], v192 offset:8512\n" \
  "ds_read_b64_tr_b16 v[106:107], v192 offset:8768\n" \
  "v_mfma_f32_16x16x32_bf16 v[212:215], v[124:127], v[148:151], v[212:215]\n" \
  "ds_read_b64_tr_b16 v[108:109], v192 offset:8544\n" \
  "ds_read_b64_tr_b16 v[110:111], v192 offset:8800\n" \
  "s_waitcnt vmcnt(8) lgkmcnt(4)\n" \
  "v_mfma_f32_16x16x32_bf16 v[200:203], v[96:99], v[152:155], v[200:203]\n" \
  "ds_read_b64_tr_b16 v[112:113], v192 offset:8960\n" \
  "ds_read_b64_tr_b16 v[114:115], v192 offset:9216\n" \
  "s_mov_b32 s53, 57344\n" \
  "buffer_load_dwordx4 v[180:183], %7, s[40:43], s53 offen offset:2048 sc1\n" \
  "v_mfma_f32_16x16x32_bf16 v[204:207], v[100:103], v[152:155], v[204:207]\n" \
  "ds_read_b64_tr_b16 v[116:117], v192 offset:8992\n" \
  "ds_read_b64_tr_b16 v[118:119], v192 offset:9248\n" \
  "s_waitcnt lgkmcnt(4)\n" \
  "v_mfma_f32_16x16x32_bf16 v[208:211], v[104:107], v[152:155], v[208:211]\n" \
  "ds_read_b64_tr_b16 v[120:121], v192 offset:9024\n" \
  "ds_read_b64_tr_b16 v[122:123], v192 offset:9280\n" \
  "v_mfma_f32_16x16x32_bf16 v[212:215], v[108:111], v[152:155], v[212:215]\n" \
  "ds_read_b64_tr_b16 v[124:125], v192 offset:9056\n" \
  "ds_read_b64_tr_b16 v[126:127], v192 offset:9312\n" \
  "s_waitcnt vmcnt(8) lgkmcnt(4)\n" \
  "v_mfma_f32_16x16x32_bf16 v[200:203], v[112:115], v[156:159], v[200:203]\n" \
  "v_add_u32 v192, 101376, %8\n" \
  "s_barrier\n" \
  "ds_read_b64_tr_b16 v[96:97], v192 offset:0\n" \
  "ds_read_b64_tr_b16 v[98:99], v192 offset:256\n" \
  "s_mov_b32 s53, 61440\n" \
  "buffer_load_dwordx4 v[184:187], %7, s[40:43], s53 offen offset:0 sc1\n" \
  "v_mfma_f32_16x16x32_bf16 v[204:207], v[116:119], v[156:159], v[204:207]\n" \
  "ds_read_b64_tr_b16 v[100:101], v192 offset:32\n" \
  "ds_read_b64_tr_b16 v[102:103], v192 offset:288\n" \
  "s_waitcnt lgkmcnt(4)\n" \
  "v_mfma_f32_16x16x32_bf16 v[208:211], v[120:123], v[156:159], v[208:211]\n" \
  "ds_read_b64_tr_b16 v[104:105], v192 offset:64\n" \
  "ds_read_b64_tr_b16 v[106:107], v192 offset:320\n" \
  "v_mfma_f32_16x16x32_bf16 v[212:215], v[124:127], v[156:159], v[212:215]\n" \
  "ds_read_b64_tr_b16 v[108:109], v192 offset:96\n" \
  "ds_read_b64_tr_b16 v[110:111], v192 offset:352\n" \
  "s_waitcnt vmcnt(8) lgkmcnt(4)\n" \
  "v_mfma_f32_16x16x32_bf16 v[200:203], v[96:99], v[160:163], v[200:203]\n" \
  "ds_read_b64_tr_b16 v[112:113], v192 offset:512\n" \
  "ds_read_b64_tr_b16 v[114:115], v192 offset:768\n" \
  "s_mov_b32 s53, 61440\n" \
  "buffer_load_dwordx4 v[188:191], %7, s[40:43], s53 offen offset:2048 sc1\n" \
  "v_mfma_f32_16x16x32_bf16 v[204:207], v[100:103], v[160:163], v[204:207]\n" \
  "ds_read_b64_tr_b16 v[116:117], v192 offset:544\n" \
  "ds_read_b64_tr_b16 v[118:119], v192 offset:800\n" \
  "s_add_u32 m0, %19, 0\n" \
  "s_mov_b32 s53, 0\n" \
  "buffer_load_dwordx4 %14, %18, s53 offen lds\n" \
  "s_waitcnt lgkmcnt(4)\n" \
  "v_mfma_f32_16x16x32_bf16 v[208:211], v[104:107], v[160:163], v[208:211]\n" \
  "ds_read_b64_tr_b16 v[120:121], v192 offset:576\n" \
  "ds_read_b64_tr_b16 v[122:123], v192 offset:832\n" \
  "v_mfma_f32_16x16x32_bf16 v[212:215], v[108:111], v[160:163], v[212:215]\n" \
  "ds_read_b64_tr_b16 v[124:125], v192 offset:608\n" \
  "ds_read_b64_tr_b16 v[126:127], v192 offset:864\n" \
  "s_waitcnt vmcnt(9) lgkmcnt(4)\n" \
  "v_mfma_f32_16x16x32_bf16 v[200:203], v[112:115], v[164:167], v[200:203]\n" \
  "ds_read_b64_tr_b16 v[96:97], v192 offset:8448\n" \
  "ds_read_b64_tr_b16 v[98:99], v192 offset:8704\n" \
  "s_add_u32 m0, %19, 4096\n" \
  "s_mov_b32 s53, 0\n" \
  "buffer_load_dwordx4 %15, %18, s53 offen lds\n" \
  "v_mfma_f32_16x16x32_bf16 v[204:207], v[116:119], v[164:167], v[204:207]\n" \
  "ds_read_b64_tr_b16 v[100:101], v192 offset:8480\n" \
  "ds_read_b64_tr_b16 v[102:103], v192 offset:8736\n" \
  "s_waitcnt lgkmcnt(4)\n" \
  "v_mfma_f32_16x16x32_bf16 v[208:211], v[120:123], v[164:167], v[208:211]\n" \
  "ds_read_b64_tr_b16 v[104:105], v192 offset:8512\n" \
  "ds_read_b64_tr_b16 v[106:107], v192 offset:8768\n" \
  "v_mfma_f32_16x16x32_bf16 v[212:215], v[124:127], v[164:167], v[212:215]\n" \
  "ds_read_b64_tr_b16 v[108:109], v192 offset:8544\n" \
  "ds_read_b64_tr_b16 v[110:111], v192 offset:8800\n" \
  "s_add_u32 m0, %19, 8192\n" \
  "s_mov_b32 s53, 0\n" \
  "buffer_load_dwordx4 %16, %18, s53 offen lds\n" \
  "s_waitcnt vmcnt(10) lgkmcnt(4)\n" \
  "v_mfma_f32_16x16x32_bf16 v[200:203], v[96:99], v[168:171], v[200:203]\n" \
  "ds_read_b64_tr_b16 v[112:113], v192 offset:8960\n" \
  "ds_read_b64_tr_b16 v[114:115], v192 offset:9216\n" \
  "v_mfma_f32_16x16x32_bf16 v[204:207], v[100:103], v[168:171], v[204:207]\n" \
  "ds_read_b64_tr_b16 v[116:117], v192 offset:8992\n" \
  "ds_read_b64_tr_b16 v[118:119], v192 offset:9248\n" \
  "s_waitcnt lgkmcnt(4)\n" \
  "v_mfma_f32_16x16x32_bf16 v[208:211], v[104:107], v[168:171], v[208:211]\n" \
  "ds_read_b64_tr_b16 v[120:121], v192 offset:9024\n" \
  "ds_read_b64_tr_b16 v[122:123], v192 offset:9280\n" \
  "s_cmp_eq_u32 %20, 0\n" \
  "s_cbranch_scc1 Lskip1_%=\n" \
  "s_add_u32 m0, %19, 12288\n" \
  "s_mov_b32 s53, 0\n" \
  "buffer_load_dwordx4 %17, %18, s53 offen lds\n" \
  "Lskip1_%=:\n" \
  "v_mfma_f32_16x16x32_bf16 v[212:215], v[108:111], v[168:171], v[212:215]\n" \
  "ds_read_b64_tr_b16 v[124:125], v192 offset:9056\n" \
  "ds_read_b64_tr_b16 v[126:127], v192 offset:9312\n" \
  "s_waitcnt vmcnt(9) lgkmcnt(4)\n" \
  "v_mfma_f32_16x16x32_bf16 v[200:203], v[112:115], v[172:175], v[200:203]\n" \
  "v_add_u32 v192, 118272, %8\n" \
  "ds_read_b64_tr_b16 v[96:97], v192 offset:0\n" \
  "ds_read_b64_tr_b16 v[98:99], v192 offset:256\n" \
  "v_mfma_f32_16x16x32_bf16 v[204:207], v[116:119], v[172:175], v[204:207]\n" \
  "ds_read_b64_tr_b16 v[100:101], v192 offset:32\n" \
  "ds_read_b64_tr_b16 v[102:103], v192 offset:288\n" \
  "s_add_u32 m0, %19, 28672\n" \
  "s_mov_b32 s53, 458752\n" \
  "buffer_load_dwordx4 %14, %18, s53 offen lds\n" \
  "s_waitcnt lgkmcnt(4)\n" \
  "v_mfma_f32_16x16x32_bf16 v[208:211], v[120:123], v[172:175], v[208:211]\n" \
  "ds_read_b64_tr_b16 v[104:105], v192 offset:64\n" \
  "ds_read_b64_tr_b16 v[106:107], v192 offset:320\n" \
  "v_mfma_f32_16x16x32_bf16 v[212:215], v[124:127], v[172:175], v[212:215]\n" \
  "ds_read_b64_tr_b16 v[108:109], v192 offset:96\n" \
  "ds_read_b64_tr_b16 v[110:111], v192 offset:352\n" \
  "s_waitcnt vmcnt(7) lgkmcnt(4)\n" \
  "v_mfma_f32_16x16x32_bf16 v[200:203], v[96:99], v[176:179], v[200:203]\n" \
  "ds_read_b64_tr_b16 v[112:113], v192 offset:512\n" \
  "ds_read_b64_tr_b16 v[114:115], v192 offset:768\n" \
  "s_add_u32 m0, %19, 32768\n" \
  "s_mov_b32 s53, 458752\n" \
  "buffer_load_dwordx4 %15, %18, s53 offen lds\n" \
  "v_mfma_f32_16x16x32_bf16 v[204:207], v[100:103], v[176:179], v[204:207]\n" \
  "ds_read_b64_tr_b16 v[116:117], v192 offset:544\n" \
  "ds_read_b64_tr_b16 v[118:119], v192 offset:800\n" \
  "s_waitcnt lgkmcnt(4)\n" \
  "v_mfma_f32_16x16x32_bf16 v[208:211], v[104:107], v[176:179], v[208:211]\n" \
  "ds_read_b64_tr_b16 v[120:121], v192 offset:576\n" \
  "ds_read_b64_tr_b16 v[122:123], v192 offset:832\n" \
  "v_mfma_f32_16x16x32_bf16 v[212:215], v[108:111], v[176:179], v[212:215]\n" \
  "ds_read_b64_tr_b16 v[124:125], v192 offset:608\n" \
  "ds_read_b64_tr_b16 v[126:127], v192 offset:864\n" \
  "s_add_u32 m0, %19, 36864\n" \
  "s_mov_b32 s53, 458752\n" \
  "buffer_load_dwordx4 %16, %18, s53 offen lds\n" \
  "s_waitcnt vmcnt(8) lgkmcnt(4)\n" \
  "v_mfma_f32_16x16x32_bf16 v[200:203], v[112:115], v[180:183], v[200:203]\n" \
  "ds_read_b64_tr_b16 v[96:97], v192 offset:8448\n" \
  "ds_read_b64_tr_b16 v[98:99], v192 offset:8704\n" \
  "v_mfma_f32_16x16x32_bf16 v[204:207], v[116:119], v[180:183], v[204:207]\n" \
  "ds_read_b64_tr_b16 v[100:101], v192 offset:8480\n" \
  "ds_read_b64_tr_b16 v[102:103], v192 offset:8736\n" \
  "s_waitcnt lgkmcnt(4)\n" \
  "v_mfma_f32_16x16x32_bf16 v[208:211], v[120:123], v[180:183], v[208:211]\n" \
  "ds_read_b64_tr_b16 v[104:105], v192 offset:8512\n" \
  "ds_read_b64_tr_b16 v[106:107], v192 offset:8768\n" \
  "s_cmp_eq_u32 %20, 0\n" \
  "s_cbranch_scc1 Lskip2_%=\n" \
  "s_add_u32 m0, %19, 40960\n" \
  "s_mov_b32 s53, 458752\n" \
  "buffer_load_dwordx4 %17, %18, s53 offen lds\n" \
  "Lskip2_%=:\n" \
  "v_mfma_f32_16x16x32_bf16 v[212:215], v[124:127], v[180:183], v[212:215]\n" \
  "ds_read_b64_tr_b16 v[108:109], v192 offset:8544\n" \
  "ds_read_b64_tr_b16 v[110:111], v192 offset:8800\n" \
  "s_waitcnt vmcnt(7) lgkmcnt(4)\n" \
  "v_mfma_f32_16x16x32_bf16 v[200:203], v[96:99], v[184:187], v[200:203]\n" \
  "ds_read_b64_tr_b16 v[112:113], v192 offset:8960\n" \
  "ds_read_b64_tr_b16 v[114:115], v192 offset:9216\n" \
  "v_mfma_f32_16x16x32_bf16 v[204:207], v[100:103], v[184:187], v[204:207]\n" \
  "ds_read_b64_tr_b16 v[116:117], v192 offset:8992\n" \
  "ds_read_b64_tr_b16 v[118:119], v192 offset:9248\n" \
  "s_waitcnt lgkmcnt(4)\n" \
  "v_mfma_f32_16x16x32_bf16 v[208:211], v[104:107], v[184:187], v[208:211]\n" \
  "ds_read_b64_tr_b16 v[120:121], v192 offset:9024\n" \
  "ds_read_b64_tr_b16 v[122:123], v192 offset:9280\n" \
  "v_mfma_f32_16x16x32_bf16 v[212:215], v[108:111], v[184:187], v[212:215]\n" \
  "ds_read_b64_tr_b16 v[124:125], v192 offset:9056\n" \
  "ds_read_b64_tr_b16 v[126:127], v192 offset:9312\n" \
  "s_waitcnt vmcnt(6) lgkmcnt(4)\n" \
  "v_mfma_f32_16x16x32_bf16 v[200:203], v[112:115], v[188:191], v[200:203]\n" \
  "v_mfma_f32_16x16x32_bf16 v[204:207], v[116:119], v[188:191], v[204:207]\n" \
  "s_waitcnt lgkmcnt(0)\n" \
  "v_mfma_f32_16x16x32_bf16 v[208:211], v[120:123], v[188:191], v[208:211]\n" \
  "v_mfma_f32_16x16x32_bf16 v[212:215], v[124:127], v[188:191], v[212:215]\n" \
  "s_waitcnt vmcnt(10)\n" \
  "s_nop 15\n"
#define K2_CLOB "v96", "v97", "v98", "v99", "v100", "v101", "v102", "v103", "v104", "v105", "v106", "v107", "v108", "v109", "v110", "v111", "v112", "v113", "v114", "v115", "v116", "v117", "v118", "v119", "v120", "v121", "v122", "v123", "v124", "v125", "v126", "v127", "v128", "v129", "v130", "v131", "v132", "v133", "v134", "v135", "v136", "v137", "v138", "v139", "v140", "v141", "v142", "v143", "v144", "v145", "v146", "v147", "v148", "v149", "v150", "v151", "v152", "v153", "v154", "v155", "v156", "v157", "v158", "v159", "v160", "v161", "v162", "v163", "v164", "v165", "v166", "v167", "v168", "v169", "v170", "v171", "v172", "v173", "v174", "v175", "v176", "v177", "v178", "v179", "v180", "v181", "v182", "v183", "v184", "v185", "v186", "v187", "v188", "v189", "v190", "v191", "v192", "s40", "s41", "s42", "s43", "s44", "s45", "s46", "s47", "s48", "s49", "s50", "s51", "s52", "s53", "scc", "memory"
#define K2_OUT(a, x) "=&{v[200:215]}"((a)[0])
#define K2_LDS 135168
#define K2_LAST 118272
#define K2_SS 16896
#define K2_NBAR 7
#define K4_PRE0 \
  "s_nop 4\n" \
  "s_mov_b64 s[44:45], %0\n" \
  "s_mov_b32 s46, %1\n" \
  "s_mov_b32 s47, 0x20000\n" \
  "s_and_b32 s45, s45, 0xffff\n" \
  "s_add_u32 m0, %2, 0\n" \
  "s_mov_b32 s53, 0\n" \
  "buffer_load_dwordx4 %4, s[44:47], s53 offen lds\n" \
  "s_add_u32 m0, %2, 4096\n" \
  "s_mov_b32 s53, 0\n" \
  "buffer_load_dwordx4 %5, s[44:47], s53 offen lds\n" \
  "s_add_u32 m0, %2, 8192\n" \
  "s_mov_b32 s53, 0\n" \
  "buffer_load_dwordx4 %6, s[44:47], s53 offen lds\n" \
  "s_cmp_eq_u32 %3, 0\n" \
  "s_cbranch_scc1 Lskip1_%=\n" \
  "s_add_u32 m0, %2, 12288\n" \
  "s_mov_b32 s53, 0\n" \
  "buffer_load_dwordx4 %7, s[44:47], s53 offen lds\n" \
  "Lskip1_%=:\n" \
  "s_add_u32 m0, %2, 28672\n" \
  "s_mov_b32 s53, 458752\n" \
  "buffer_load_dwordx4 %4, s[44:47], s53 offen lds\n" \
  "s_add_u32 m0, %2, 32768\n" \
  "s_mov_b32 s53, 458752\n" \
  "buffer_load_dwordx4 %5, s[44:47], s53 offen lds\n" \
  "s_add_u32 m0, %2, 36864\n" \
  "s_mov_b32 s53, 458752\n" \
  "buffer_load_dwordx4 %6, s[44:47], s53 offen lds\n" \
  "s_cmp_eq_u32 %3, 0\n" \
  "s_cbranch_scc1 Lskip2_%=\n" \
  "s_add_u32 m0, %2, 40960\n" \
  "s_mov_b32 s53, 458752\n" \
  "buffer_load_dwordx4 %7, s[44:47], s53 offen lds\n" \
  "Lskip2_%=:\n"
#define K4_NPRE 6
#define K4_PRECLOB "s40", "s41", "s42", "s43", "s44", "s45", "s46", "s47", "s48", "s49", "s50", "s51", "s52", "s53", "scc", "memory"
#define K4_ASM \
  "s_nop 4\n" \
  "s_mov_b64 s[44:45], %9\n" \
  "s_mov_b32 s46, %10\n" \
  "s_mov_b32 s47, 0x20000\n" \
  "s_mov_b64 s[40:41], %7\n" \
  "s_mov_b32 s42, %8\n" \
  "s_mov_b32 s43, 0x20000\n" \
  "s_and_b32 s41, s41, 0xffff\n" \
  "s_and_b32 s45, s45, 0xffff\n" \
  "s_mov_b32 s53, 0\n" \
  "buffer_load_dwordx4 v[108:111], %13, s[40:43], s53 offen offset:0 sc1\n" \
  "s_mov_b32 s53, 0\n" \
  "buffer_load_dwordx4 v[112:115], %13, s[40:43], s53 offen offset:1024 sc1\n" \
  "s_mov_b32 s53, 0\n" \
  "buffer_load_dwordx4 v[116:119], %13, s[40:43], s53 offen offset:2048 sc1\n" \
  "s_mov_b32 s53, 0\n" \
  "buffer_load_dwordx4 v[120:123], %13, s[40:43], s53 offen offset:3072 sc1\n" \
  "s_mov_b32 s53, 4096\n" \
  "buffer_load_dwordx4 v[124:127], %13, s[40:43], s53 offen offset:0 sc1\n" \
  "s_mov_b32 s53, 4096\n" \
  "buffer_load_dwordx4 v[128:131], %13, s[40:43], s53 offen offset:1024 sc1\n" \
  "s_add_u32 m0, %11, 57344\n" \
  "s_mov_b32 s53, 917504\n" \
  "buffer_load_dwordx4 %15, s[44:47], s53 offen lds\n" \
  "s_mov_b32 s48, 12288\n" \
  "s_mov_b32 s49, 1835008\n" \
  "s_mov_b32 s50, 57344\n" \
  "s_add_u32 s51, %11, 114688\n" \
  "s_mov_b32 s52, 5\n" \
  "v_add_u32 v140, 0, %14\n" \
  "s_waitcnt lgkmcnt(0)\n" \
  "ds_read_b64_tr_b16 v[76:77], v140 offset:0\n" \
  "ds_read_b64_tr_b16 v[78:79], v140 offset:3584\n" \
  "ds_read_b64_tr_b16 v[80:81], v140 offset:14336\n" \
  "ds_read_b64_tr_b16 v[82:83], v140 offset:17920\n" \
  "ds_read_b64_tr_b16 v[84:85], v140 offset:32\n" \
  "ds_read_b64_tr_b16 v[86:87], v140 offset:3616\n" \
  "ds_read_b64_tr_b16 v[88:89], v140 offset:14368\n" \
  "ds_read_b64_tr_b16 v[90:91], v140 offset:17952\n" \
  "s_waitcnt vmcnt(5) lgkmcnt(4)\n" \
  "v_mfma_f32_16x16x32_bf16 v[144:147], v[76:79], v[108:111], 0\n" \
  "ds_read_b64_tr_b16 v[92:93], v140 offset:64\n" \
  "v_mfma_f32_16x16x32_bf16 v[148:151], v[76:79], v[112:115], 0\n" \
  "ds_read_b64_tr_b16 v[94:95], v140 offset:3648\n" \
  "s_add_u32 m0, %11, 61440\n" \
  "s_mov_b32 s53, 917504\n" \
  "buffer_load_dwordx4 %16, s[44:47], s53 offen lds\n" \
  "v_mfma_f32_16x16x32_bf16 v[152:155], v[80:83], v[108:111], 0\n" \
  "ds_read_b64_tr_b16 v[96:97], v140 offset:14400\n" \
  "v_mfma_f32_16x16x32_bf16 v[156:159], v[80:83], v[112:115], 0\n" \
  "ds_read_b64_tr_b16 v[98:99], v140 offset:17984\n" \
  "s_waitcnt lgkmcnt(4)\n" \
  "v_mfma_f32_16x16x32_bf16 v[160:163], v[84:87], v[108:111], 0\n" \
  "ds_read_b64_tr_b16 v[100:101], v140 offset:96\n" \
  "v_mfma_f32_16x16x32_bf16 v[164:167], v[84:87], v[112:115], 0\n" \
  "ds_read_b64_tr_b16 v[102:103], v140 offset:3680\n" \
  "v_mfma_f32_16x16x32_bf16 v[168:171], v[88:91], v[108:111], 0\n" \
  "ds_read_b64_tr_b16 v[104:105], v140 offset:14432\n" \
  "v_mfma_f32_16x16x32_bf16 v[172:175], v[88:91], v[112:115], 0\n" \
  "ds_read_b64_tr_b16 v[106:107], v140 offset:18016\n" \
  "s_waitcnt lgkmcnt(4)\n" \
  "v_mfma_f32_16x16x32_bf16 v[176:179], v[92:95], v[108:111], 0\n" \
  "ds_read_b64_tr_b16 v[76:77], v140 offset:128\n" \
  "v_mfma_f32_16x16x32_bf16 v[180:183], v[92:95], v[112:115], 0\n" \
  "ds_read_b64_tr_b16 v[78:79], v140 offset:3712\n" \
  "v_mfma_f32_16x16x32_bf16 v[184:187], v[96:99], v[108:111], 0\n" \
  "ds_read_b64_tr_b16 v[80:81], v140 offset:14464\n" \
  "v_mfma_f32_16x16x32_bf16 v[188:191], v[96:99], v[112:115], 0\n" \
  "ds_read_b64_tr_b16 v[82:83], v140 offset:18048\n" \
  "s_add_u32 m0, %11, 65536\n" \
  "s_mov_b32 s53, 917504\n" \
  "buffer_load_dwordx4 %17, s[44:47], s53 offen lds\n" \
  "s_waitcnt lgkmcnt(4)\n" \
  "v_mfma_f32_16x16x32_bf16 v[192:195], v[100:103], v[108:111], 0\n" \
  "ds_read_b64_tr_b16 v[84:85], v140 offset:160\n" \
  "v_mfma_f32_16x16x32_bf16 v[196:199], v[100:103], v[112:115], 0\n" \
  "ds_read_b64_tr_b16 v[86:87], v140 offset:3744\n" \
  "s_mov_b32 s53, 4096\n" \
  "buffer_load_dwordx4 v[132:135], %13, s[40:43], s53 offen offset:2048 sc1\n" \
  "v_mfma_f32_16x16x32_bf16 v[200:203], v[104:107], v[108:111], 0\n" \
  "ds_read_b64_tr_b16 v[88:89], v140 offset:14496\n" \
  "v_mfma_f32_16x16x32_bf16 v[204:207], v[104:107], v[112:115], 0\n" \
  "ds_read_b64_tr_b16 v[90:91], v140 offset:18080\n" \
  "s_mov_b32 s53, 4096\n" \
  "buffer_load_dwordx4 v[136:139], %13, s[40:43], s53 offen offset:3072 sc1\n" \
  "s_waitcnt lgkmcnt(4)\n" \
  "v_mfma_f32_16x16x32_bf16 v[208:211], v[76:79], v[108:111], 0\n" \
  "ds_read_b64_tr_b16 v[92:93], v140 offset:192\n" \
  "v_mfma_f32_16x16x32_bf16 v[212:215], v[76:79], v[112:115], 0\n" \
  "ds_read_b64_tr_b16 v[94:95], v140 offset:3776\n" \
  "v_mfma_f32_16x16x32_bf16 v[216:219], v[80:83], v[108:111], 0\n" \
  "ds_read_b64_tr_b16 v[96:97], v140 offset:14528\n" \
  "v_mfma_f32_16x16x32_bf16 v[220:223], v[80:83], v[112:115], 0\n" \
  "ds_read_b64_tr_b16 v[98:99], v140 offset:18112\n" \
  "s_cmp_eq_u32 %12, 0\n" \
  "s_cbranch_scc1 Lskip3_%=\n" \
  "s_add_u32 m0, %11, 69632\n" \
  "s_mov_b32 s53, 917504\n" \
  "buffer_load_dwordx4 %18, s[44:47], s53 offen lds\n" \
  "Lskip3_%=:\n" \
  "s_waitcnt lgkmcnt(4)\n" \
  "v_mfma_f32_16x16x32_bf16 v[224:227], v[84:87], v[108:111], 0\n" \
  "s_setprio 2\n" \
  "ds_read_b64_tr_b16 v[100:101], v140 offset:7168\n" \
  "v_mfma_f32_16x16x32_bf16 v[228:231], v[84:87], v[112:115], 0\n" \
  "ds_read_b64_tr_b16 v[102:103], v140 offset:10752\n" \
  "v_mfma_f32_16x16x32_bf16 v[232:235], v[88:91], v[108:111], 0\n" \
  "ds_read_b64_tr_b16 v[104:105], v140 offset:21504\n" \
  "v_mfma_f32_16x16x32_bf16 v[236:239], v[88:91], v[112:115], 0\n" \
  "ds_read_b64_tr_b16 v[106:107], v140 offset:25088\n" \
  "s_waitcnt lgkmcnt(4)\n" \
  "v_mfma_f32_16x16x32_bf16 v[240:243], v[92:95], v[108:111], 0\n" \
  "ds_read_b64_tr_b16 v[76:77], v140 offset:7200\n" \
  "v_mfma_f32_16x16x32_bf16 v[244:247], v[92:95], v[112:115], 0\n" \
  "ds_read_b64_tr_b16 v[78:79], v140 offset:10784\n" \
  "v_mfma_f32_16x16x32_bf16 v[248:251], v[96:99], v[108:111], 0\n" \
  "ds_read_b64_tr_b16 v[80:81], v140 offset:21536\n" \
  "v_mfma_f32_16x16x32_bf16 v[252:255], v[96:99], v[112:115], 0\n" \
  "ds_read_b64_tr_b16 v[82:83], v140 offset:25120\n" \
  "s_waitcnt vmcnt(7) lgkmcnt(4)\n" \
  "v_mfma_f32_16x16x32_bf16 v[144:147], v[100:103], v[116:119], v[144:147]\n" \
  "ds_read_b64_tr_b16 v[84:85], v140 offset:7232\n" \
  "v_mfma_f32_16x16x32_bf16 v[148:151], v[100:103], v[120:123], v[148:151]\n" \
  "ds_read_b64_tr_b16 v[86:87], v140 offset:10816\n" \
  "s_add_u32 m0, %11, 86016\n" \
  "s_mov_b32 s53, 1376256\n" \
  "buffer_load_dwordx4 %15, s[44:47], s53 offen lds\n" \
  "v_mfma_f32_16x16x32_bf16 v[152:155], v[104:107], v[116:119], v[152:155]\n" \
  "ds_read_b64_tr_b16 v[88:89], v140 offset:21568\n" \
  "v_mfma_f32_16x16x32_bf16 v[156:159], v[104:107], v[120:123], v[156:159]\n" \
  "ds_read_b64_tr_b16 v[90:91], v140 offset:25152\n" \
  "s_waitcnt lgkmcnt(4)\n" \
  "v_mfma_f32_16x16x32_bf16 v[160:163], v[76:79], v[116:119], v[160:163]\n" \
  "ds_read_b64_tr_b16 v[92:93], v140 offset:7264\n" \
  "v_mfma_f32_16x16x32_bf16 v[164:167], v[76:79], v[120:123], v[164:167]\n" \
  "ds_read_b64_tr_b16 v[94:95], v140 offset:10848\n" \
  "v_mfma_f32_16x16x32_bf16 v[168:171], v[80:83], v[116:119], v[168:171]\n" \
  "ds_read_b64_tr_b16 v[96:97], v140 offset:21600\n" \
  "v_mfma_f32_16x16x32_bf16 v[172:175], v[80:83], v[120:123], v[172:175]\n" \
  "ds_read_b64_tr_b16 v[98:99], v140 offset:25184\n" \
  "s_waitcnt lgkmcnt(4)\n" \
  "v_mfma_f32_16x16x32_bf16 v[176:179], v[84:87], v[116:119], v[176:179]\n" \
  "ds_read_b64_tr_b16 v[100:101], v140 offset:7296\n" \
  "v_mfma_f32_16x16x32_bf16 v[180:183], v[84:87], v[120:123], v[180:183]\n" \
  "ds_read_b64_tr_b16 v[102:103], v140 offset:10880\n" \
  "s_add_u32 m0, %11, 90112\n" \
  "s_mov_b32 s53, 1376256\n" \
  "buffer_load_dwordx4 %16, s[44:47], s53 offen lds\n" \
  "v_mfma_f32_16x16x32_bf16 v[184:187], v[88:91], v[116:119], v[184:187]\n" \
  "ds_read_b64_tr_b16 v[104:105], v140 offset:21632\n" \
  "v_mfma_f32_16x16x32_bf16 v[188:191], v[88:91], v[120:123], v[188:191]\n" \
  "ds_read_b64_tr_b16 v[106:107], v140 offset:25216\n" \
  "s_waitcnt lgkmcnt(4)\n" \
  "v_mfma_f32_16x16x32_bf16 v[192:195], v[92:95], v[116:119], v[192:195]\n" \
  "ds_read_b64_tr_b16 v[76:77], v140 offset:7328\n" \
  "v_mfma_f32_16x16x32_bf16 v[196:199], v[92:95], v[120:123], v[196:199]\n" \
  "ds_read_b64_tr_b16 v[78:79], v140 offset:10912\n" \
  "s_mov_b32 s53, 8192\n" \
  "buffer_load_dwordx4 v[108:111], %13, s[40:43], s53 offen offset:0 sc1\n" \
  "v_mfma_f32_16x16x32_bf16 v[200:203], v[96:99], v[116:119], v[200:203]\n" \
  "ds_read_b64_tr_b16 v[80:81], v140 offset:21664\n" \
  "v_mfma_f32_16x16x32_bf16 v[204:207], v[96:99], v[120:123], v[204:207]\n" \
  "ds_read_b64_tr_b16 v[82:83], v140 offset:25248\n" \
  "s_mov_b32 s53, 8192\n" \
  "buffer_load_dwordx4 v[112:115], %13, s[40:43], s53 offen offset:1024 sc1\n" \
  "s_waitcnt lgkmcnt(4)\n" \
  "v_mfma_f32_16x16x32_bf16 v[208:211], v[100:103], v[116:119], v[208:211]\n" \
  "ds_read_b64_tr_b16 v[84:85], v140 offset:7360\n" \
  "v_mfma_f32_16x16x32_bf16 v[212:215], v[100:103], v[120:123], v[212:215]\n" \
  "ds_read_b64_tr_b16 v[86:87], v140 offset:10944\n" \
  "v_mfma_f32_16x16x32_bf16 v[216:219], v[104:107], v[116:119], v[216:219]\n" \
  "ds_read_b64_tr_b16 v[88:89], v140 offset:21696\n" \
  "v_mfma_f32_16x16x32_bf16 v[220:223], v[104:107], v[120:123], v[220:223]\n" \
  "ds_read_b64_tr_b16 v[90:91], v140 offset:25280\n" \
  "s_add_u32 m0, %11, 94208\n" \
  "s_mov_b32 s53, 1376256\n" \
  "buffer_load_dwordx4 %17, s[44:47], s53 offen lds\n" \
  "s_waitcnt lgkmcnt(4)\n" \
  "v_mfma_f32_16x16x32_bf16 v[224:227], v[76:79], v[116:119], v[224:227]\n" \
  "v_add_u32 v140, 28672, %14\n" \
  "s_setprio 1\n" \
  "ds_read_b64_tr_b16 v[92:93], v140 offset:0\n" \
  "v_mfma_f32_16x16x32_bf16 v[228:231], v[76:79], v[120:123], v[228:231]\n" \
  "ds_read_b64_tr_b16 v[94:95], v140 offset:3584\n" \
  "v_mfma_f32_16x16x32_bf16 v[232:235], v[80:83], v[116:119], v[232:235]\n" \
  "ds_read_b64_tr_b16 v[96:97], v140 offset:14336\n" \
  "v_mfma_f32_16x16x32_bf16 v[236:239], v[80:83], v[120:123], v[236:239]\n" \
  "ds_read_b64_tr_b16 v[98:99], v140 offset:17920\n" \
  "s_waitcnt lgkmcnt(4)\n" \
  "v_mfma_f32_16x16x32_bf16 v[240:243], v[84:87], v[116:119], v[240:243]\n" \
  "ds_read_b64_tr_b16 v[100:101], v140 offset:32\n" \
  "v_mfma_f32_16x16x32_bf16 v[244:247], v[84:87], v[120:123], v[244:247]\n" \
  "ds_read_b64_tr_b16 v[102:103], v140 offset:3616\n" \
  "v_mfma_f32_16x16x32_bf16 v[248:251], v[88:91], v[116:119], v[248:251]\n" \
  "ds_read_b64_tr_b16 v[104:105], v140 offset:14368\n" \
  "v_mfma_f32_16x16x32_bf16 v[252:255], v[88:91], v[120:123], v[252:255]\n" \
  "ds_read_b64_tr_b16 v[106:107], v140 offset:17952\n" \
  "s_waitcnt vmcnt(10) lgkmcnt(4)\n" \
  "v_mfma_f32_16x16x32_bf16 v[144:147], v[92:95], v[124:127], v[144:147]\n" \
  "ds_read_b64_tr_b16 v[76:77], v140 offset:64\n" \
  "v_mfma_f32_16x16x32_bf16 v[148:151], v[92:95], v[128:131], v[148:151]\n" \
  "ds_read_b64_tr_b16 v[78:79], v140 offset:3648\n" \
  "s_cmp_eq_u32 %12, 0\n" \
  "s_cbranch_scc1 Lskip4_%=\n" \
  "s_add_u32 m0, %11, 98304\n" \
  "s_mov_b32 s53, 1376256\n" \
  "buffer_load_dwordx4 %18, s[44:47], s53 offen lds\n" \
  "Lskip4_%=:\n" \
  "v_mfma_f32_16x16x32_bf16 v[152:155], v[96:99], v[124:127], v[152:155]\n" \
  "ds_read_b64_tr_b16 v[80:81], v140 offset:14400\n" \
  "v_mfma_f32_16x16x32_bf16 v[156:159], v[96:99], v[128:131], v[156:159]\n" \
  "ds_read_b64_tr_b16 v[82:83], v140 offset:17984\n" \
  "s_waitcnt lgkmcnt(4)\n" \
  "v_mfma_f32_16x16x32_bf16 v[160:163], v[100:103], v[124:127], v[160:163]\n" \
  "ds_read_b64_tr_b16 v[84:85], v140 offset:96\n" \
  "v_mfma_f32_16x16x32_bf16 v[164:167], v[100:103], v[128:131], v[164:167]\n" \
  "ds_read_b64_tr_b16 v[86:87], v140 offset:3680\n" \
  "v_mfma_f32_16x16x32_bf16 v[168:171], v[104:107], v[124:127], v[168:171]\n" \
  "ds_read_b64_tr_b16 v[88:89], v140 offset:14432\n" \
  "v_mfma_f32_16x16x32_bf16 v[172:175], v[104:107], v[128:131], v[172:175]\n" \
  "ds_read_b64_tr_b16 v[90:91], v140 offset:18016\n" \
  "s_waitcnt lgkmcnt(4)\n" \
  "v_mfma_f32_16x16x32_bf16 v[176:179], v[76:79], v[124:127], v[176:179]\n" \
  "ds_read_b64_tr_b16 v[92:93], v140 offset:128\n" \
  "v_mfma_f32_16x16x32_bf16 v[180:183], v[76:79], v[128:131], v[180:183]\n" \
  "ds_read_b64_tr_b16 v[94:95], v140 offset:3712\n" \
  "v_mfma_f32_16x16x32_bf16 v[184:187], v[80:83], v[124:127], v[184:187]\n" \
  "ds_read_b64_tr_b16 v[96:97], v140 offset:14464\n" \
  "v_mfma_f32_16x16x32_bf16 v[188:191], v[80:83], v[128:131], v[188:191]\n" \
  "ds_read_b64_tr_b16 v[98:99], v140 offset:18048\n" \
  "s_waitcnt lgkmcnt(4)\n" \
  "v_mfma_f32_16x16x32_bf16 v[192:195], v[84:87], v[124:127], v[192:195]\n" \
  "ds_read_b64_tr_b16 v[100:101], v140 offset:160\n" \
  "v_mfma_f32_16x16x32_bf16 v[196:199], v[84:87], v[128:131], v[196:199]\n" \
  "ds_read_b64_tr_b16 v[102:103], v140 offset:3744\n" \
  "s_mov_b32 s53, 8192\n" \
  "buffer_load_dwordx4 v[116:119], %13, s[40:43], s53 offen offset:2048 sc1\n" \
  "v_mfma_f32_16x16x32_bf16 v[200:203], v[88:91], v[124:127], v[200:203]\n" \
  "ds_read_b64_tr_b16 v[104:105], v140 offset:14496\n" \
  "v_mfma_f32_16x16x32_bf16 v[204:207], v[88:91], v[128:131], v[204:207]\n" \
  "ds_read_b64_tr_b16 v[106:107], v140 offset:18080\n" \
  "s_mov_b32 s53, 8192\n" \
  "buffer_load_dwordx4 v[120:123], %13, s[40:43], s53 offen offset:3072 sc1\n" \
  "s_waitcnt lgkmcnt(4)\n" \
  "v_mfma_f32_16x16x32_bf16 v[208:211], v[92:95], v[124:127], v[208:211]\n" \
  "ds_read_b64_tr_b16 v[76:77], v140 offset:192\n" \
  "v_mfma_f32_16x16x32_bf16 v[212:215], v[92:95], v[128:131], v[212:215]\n" \
  "ds_read_b64_tr_b16 v[78:79], v140 offset:3776\n" \
  "v_mfma_f32_16x16x32_bf16 v[216:219], v[96:99], v[124:127], v[216:219]\n" \
  "ds_read_b64_tr_b16 v[80:81], v140 offset:14528\n" \
  "v_mfma_f32_16x16x32_bf16 v[220:223], v[96:99], v[128:131], v[220:223]\n" \
  "ds_read_b64_tr_b16 v[82:83], v140 offset:18112\n" \
  "s_waitcnt lgkmcnt(4)\n" \
  "v_mfma_f32_16x16x32_bf16 v[224:227], v[100:103], v[124:127], v[224:227]\n" \
  "s_setprio 0\n" \
  "ds_read_b64_tr_b16 v[84:85], v140 offset:7168\n" \
  "v_mfma_f32_16x16x32_bf16 v[228:231], v[100:103], v[128:131], v[228:231]\n" \
  "ds_read_b64_tr_b16 v[86:87], v140 offset:10752\n" \
  "v_mfma_f32_16x16x32_bf16 v[232:235], v[104:107], v[124:127], v[232:235]\n" \
  "ds_read_b64_tr_b16 v[88:89], v140 offset:21504\n" \
  "v_mfma_f32_16x16x32_bf16 v[236:239], v[104:107], v[128:131], v[236:239]\n" \
  "ds_read_b64_tr_b16 v[90:91], v140 offset:25088\n" \
  "s_waitcnt lgkmcnt(4)\n" \
  "v_mfma_f32_16x16x32_bf16 v[240:243], v[76:79], v[124:127], v[240:243]\n" \
  "ds_read_b64_tr_b16 v[92:93], v140 offset:7200\n" \
  "v_mfma_f32_16x16x32_bf16 v[244:247], v[76:79], v[128:131], v[244:247]\n" \
  "ds_read_b64_tr_b16 v[94:95], v140 offset:10784\n" \
  "v_mfma_f32_16x16x32_bf16 v[248:251], v[80:83], v[124:127], v[248:251]\n" \
  "ds_read_b64_tr_b16 v[96:97], v140 offset:21536\n" \
  "v_mfma_f32_16x16x32_bf16 v[252:255], v[80:83], v[128:131], v[252:255]\n" \
  "ds_read_b64_tr_b16 v[98:99], v140 offset:25120\n" \
  "s_waitcnt vmcnt(7) lgkmcnt(4)\n" \
  "v_mfma_f32_16x16x32_bf16 v[144:147], v[84:87], v[132:135], v[144:147]\n" \
  "ds_read_b64_tr_b16 v[100:101], v140 offset:7232\n" \
  "v_mfma_f32_16x16x32_bf16 v[148:151], v[84:87], v[136:139], v[148:151]\n" \
  "ds_read_b64_tr_b16 v[102:103], v140 offset:10816\n" \
  "v_mfma_f32_16x16x32_bf16 v[152:155], v[88:91], v[132:135], v[152:155]\n" \
  "ds_read_b64_tr_b16 v[104:105], v140 offset:21568\n" \
  "v_mfma_f32_16x16x32_bf16 v[156:159], v[88:91], v[136:139], v[156:159]\n" \
  "ds_read_b64_tr_b16 v[106:107], v140 offset:25152\n" \
  "s_waitcnt lgkmcnt(4)\n" \
  "v_mfma_f32_16x16x32_bf16 v[160:163], v[92:95], v[132:135], v[160:163]\n" \
  "ds_read_b64_tr_b16 v[76:77], v140 offset:7264\n" \
  "v_mfma_f32_16x16x32_bf16 v[164:167], v[92:95], v[136:139], v[164:167]\n" \
  "ds_read_b64_tr_b16 v[78:79], v140 offset:10848\n" \
  "v_mfma_f32_16x16x32_bf16 v[168:171], v[96:99], v[132:135], v[168:171]\n" \
  "ds_read_b64_tr_b16 v[80:81], v140 offset:21600\n" \
  "v_mfma_f32_16x16x32_bf16 v[172:175], v[96:99], v[136:139], v[172:175]\n" \
  "ds_read_b64_tr_b16 v[82:83], v140 offset:25184\n" \
  "s_waitcnt lgkmcnt(4)\n" \
  "v_mfma_f32_16x16x32_bf16 v[176:179], v[100:103], v[132:135], v[176:179]\n" \
  "ds_read_b64_tr_b16 v[84:85], v140 offset:7296\n" \
  "v_mfma_f32_16x16x32_bf16 v[180:183], v[100:103], v[136:139], v[180:183]\n" \
  "ds_read_b64_tr_b16 v[86:87], v140 offset:10880\n" \
  "v_mfma_f32_16x16x32_bf16 v[184:187], v[104:107], v[132:135], v[184:187]\n" \
  "ds_read_b64_tr_b16 v[88:89], v140 offset:21632\n" \
  "v_mfma_f32_16x16x32_bf16 v[188:191], v[104:107], v[136:139], v[188:191]\n" \
  "ds_read_b64_tr_b16 v[90:91], v140 offset:25216\n" \
  "s_waitcnt lgkmcnt(4)\n" \
  "v_mfma_f32_16x16x32_bf16 v[192:195], v[76:79], v[132:135], v[192:195]\n" \
  "ds_read_b64_tr_b16 v[92:93], v140 offset:7328\n" \
  "v_mfma_f32_16x16x32_bf16 v[196:199], v[76:79], v[136:139], v[196:199]\n" \
  "ds_read_b64_tr_b16 v[94:95], v140 offset:10912\n" \
  "s_mov_b32 s53, 12288\n" \
  "buffer_load_dwordx4 v[124:127], %13, s[40:43], s53 offen offset:0 sc1\n" \
  "v_mfma_f32_16x16x32_bf16 v[200:203], v[80:83], v[132:135], v[200:203]\n" \
  "ds_read_b64_tr_b16 v[96:97], v140 offset:21664\n" \
  "v_mfma_f32_16x16x32_bf16 v[204:207], v[80:83], v[136:139], v[204:207]\n" \
  "ds_read_b64_tr_b16 v[98:99], v140 offset:25248\n" \
  "s_mov_b32 s53, 12288\n" \
  "buffer_load_dwordx4 v[128:131], %13, s[40:43], s53 offen offset:1024 sc1\n" \
  "s_waitcnt lgkmcnt(4)\n" \
  "v_mfma_f32_16x16x32_bf16 v[208:211], v[84:87], v[132:135], v[208:211]\n" \
  "ds_read_b64_tr_b16 v[100:101], v140 offset:7360\n" \
  "v_mfma_f32_16x16x32_bf16 v[212:215], v[84:87], v[136:139], v[212:215]\n" \
  "ds_read_b64_tr_b16 v[102:103], v140 offset:10944\n" \
  "v_mfma_f32_16x16x32_bf16 v[216:219], v[88:91], v[132:135], v[216:219]\n" \
  "ds_read_b64_tr_b16 v[104:105], v140 offset:21696\n" \
  "v_mfma_f32_16x16x32_bf16 v[220:223], v[88:91], v[136:139], v[220:223]\n" \
  "ds_read_b64_tr_b16 v[106:107], v140 offset:25280\n" \
  "s_waitcnt lgkmcnt(4)\n" \
  "v_mfma_f32_16x16x32_bf16 v[224:227], v[92:95], v[132:135], v[224:227]\n" \
  "v_add_u32 v140, 57344, %14\n" \
  "s_waitcnt vmcnt(4)\n" \
  "s_barrier\n" \
  "s_setprio 3\n" \
  "ds_read_b64_tr_b16 v[76:77], v140 offset:0\n" \
  "v_mfma_f32_16x16x32_bf16 v[228:231], v[92:95], v[136:139], v[228:231]\n" \
  "ds_read_b64_tr_b16 v[78:79], v140 offset:3584\n" \
  "s_add_u32 m0, %11, 114688\n" \
  "s_mov_b32 s53, 1835008\n" \
  "buffer_load_dwordx4 %15, s[44:47], s53 offen lds\n" \
  "v_mfma_f32_16x16x32_bf16 v[232:235], v[96:99], v[132:135], v[232:235]\n" \
  "ds_read_b64_tr_b16 v[80:81], v140 offset:14336\n" \
  "v_mfma_f32_16x16x32_bf16 v[236:239], v[96:99], v[136:139], v[236:239]\n" \
  "ds_read_b64_tr_b16 v[82:83], v140 offset:17920\n" \
  "s_waitcnt lgkmcnt(4)\n" \
  "v_mfma_f32_16x16x32_bf16 v[240:243], v[100:103], v[132:135], v[240:243]\n" \
  "ds_read_b64_tr_b16 v[84:85], v140 offset:32\n" \
  "v_mfma_f32_16x16x32_bf16 v[244:247], v[100:103], v[136:139], v[244:247]\n" \
  "ds_read_b64_tr_b16 v[86:87], v140 offset:3616\n" \
  "v_mfma_f32_16x16x32_bf16 v[248:251], v[104:107], v[132:135], v[248:251]\n" \
  "ds_read_b64_tr_b16 v[88:89], v140 offset:14368\n" \
  "v_mfma_f32_16x16x32_bf16 v[252:255], v[104:107], v[136:139], v[252:255]\n" \
  "ds_read_b64_tr_b16 v[90:91], v140 offset:17952\n" \
  "Lloop_%=:\n" \
  "s_waitcnt vmcnt(6) lgkmcnt(4)\n" \
  "v_mfma_f32_16x16x32_bf16 v[144:147], v[76:79], v[108:111], v[144:147]\n" \
  "ds_read_b64_tr_b16 v[92:93], v140 offset:64\n" \
  "v_mfma_f32_16x16x32_bf16 v[148:151], v[76:79], v[112:115], v[148:151]\n" \
  "ds_read_b64_tr_b16 v[94:95], v140 offset:3648\n" \
  "s_add_u32 m0, s51, 4096\n" \
  "s_nop 0\n" \
  "buffer_load_dwordx4 %16, s[44:47], s49 offen lds\n" \
  "v_mfma_f32_16x16x32_bf16 v[152:155], v[80:83], v[108:111], v[152:155]\n" \
  "ds_read_b64_tr_b16 v[96:97], v140 offset:14400\n" \
  "v_mfma_f32_16x16x32_bf16 v[156:159], v[80:83], v[112:115], v[156:159]\n" \
  "ds_read_b64_tr_b16 v[98:99], v140 offset:17984\n" \
  "s_waitcnt lgkmcnt(4)\n" \
  "v_mfma_f32_16x16x32_bf16 v[160:163], v[84:87], v[108:111], v[160:163]\n" \
  "ds_read_b64_tr_b16 v[100:101], v140 offset:96\n" \
  "v_mfma_f32_16x16x32_bf16 v[164:167], v[84:87], v[112:115], v[164:167]\n" \
  "ds_read_b64_tr_b16 v[102:103], v140 offset:3680\n" \
  "v_mfma_f32_16x16x32_bf16 v[168:171], v[88:91], v[108:111], v[168:171]\n" \
  "ds_read_b64_tr_b16 v[104:105], v140 offset:14432\n" \
  "v_mfma_f32_16x16x32_bf16 v[172:175], v[88:91], v[112:115], v[172:175]\n" \
  "ds_read_b64_tr_b16 v[106:107], v140 offset:18016\n" \
  "s_waitcnt lgkmcnt(4)\n" \
  "v_mfma_f32_16x16x32_bf16 v[176:179], v[92:95], v[108:111], v[176:179]\n" \
  "ds_read_b64_tr_b16 v[76:77], v140 offset:128\n" \
  "v_mfma_f32_16x16x32_bf16 v[180:183], v[92:95], v[112:115], v[180:183]\n" \
  "ds_read_b64_tr_b16 v[78:79], v140 offset:3712\n" \
  "v_mfma_f32_16x16x32_bf16 v[184:187], v[96:99], v[108:111], v[184:187]\n" \
  "ds_read_b64_tr_b16 v[80:81], v140 offset:14464\n" \
  "v_mfma_f32_16x16x32_bf16 v[188:191], v[96:99], v[112:115], v[188:191]\n" \
  "ds_read_b64_tr_b16 v[82:83], v140 offset:18048\n" \
  "s_add_u32 m0, s51, 8192\n" \
  "s_nop 0\n" \
  "buffer_load_dwordx4 %17, s[44:47], s49 offen lds\n" \
  "s_waitcnt lgkmcnt(4)\n" \
  "v_mfma_f32_16x16x32_bf16 v[192:195], v[100:103], v[108:111], v[192:195]\n" \
  "ds_read_b64_tr_b16 v[84:85], v140 offset:160\n" \
  "v_mfma_f32_16x16x32_bf16 v[196:199], v[100:103], v[112:115], v[196:199]\n" \
  "ds_read_b64_tr_b16 v[86:87], v140 offset:3744\n" \
  "buffer_load_dwordx4 v[132:135], %13, s[40:43], s48 offen offset:2048 sc1\n" \
  "v_mfma_f32_16x16x32_bf16 v[200:203], v[104:107], v[108:111], v[200:203]\n" \
  "ds_read_b64_tr_b16 v[88:89], v140 offset:14496\n" \
  "v_mfma_f32_16x16x32_bf16 v[204:207], v[104:107], v[112:115], v[204:207]\n" \
  "ds_read_b64_tr_b16 v[90:91], v140 offset:18080\n" \
  "buffer_load_dwordx4 v[136:139], %13, s[40:43], s48 offen offset:3072 sc1\n" \
  "s_add_u32 s48, s48, 4096\n" \
  "s_waitcnt lgkmcnt(4)\n" \
  "v_mfma_f32_16x16x32_bf16 v[208:211], v[76:79], v[108:111], v[208:211]\n" \
  "ds_read_b64_tr_b16 v[92:93], v140 offset:192\n" \
  "v_mfma_f32_16x16x32_bf16 v[212:215], v[76:79], v[112:115], v[212:215]\n" \
  "ds_read_b64_tr_b16 v[94:95], v140 offset:3776\n" \
  "v_mfma_f32_16x16x32_bf16 v[216:219], v[80:83], v[108:111], v[216:219]\n" \
  "ds_read_b64_tr_b16 v[96:97], v140 offset:14528\n" \
  "v_mfma_f32_16x16x32_bf16 v[220:223], v[80:83], v[112:115], v[220:223]\n" \
  "ds_read_b64_tr_b16 v[98:99], v140 offset:18112\n" \
  "s_cmp_eq_u32 %12, 0\n" \
  "s_cbranch_scc1 Lskip5_%=\n" \
  "s_add_u32 m0, s51, 12288\n" \
  "s_nop 0\n" \
  "buffer_load_dwordx4 %18, s[44:47], s49 offen lds\n" \
  "Lskip5_%=:\n" \
  "s_add_u32 s49, s49, 458752\n" \
  "s_add_u32 s51, s51, 28672\n" \
  "s_sub_u32 s53, s51, %11\n" \
  "s_cmp_eq_u32 s53, 143360\n" \
  "s_cselect_b32 s51, %11, s51\n" \
  "s_waitcnt lgkmcnt(4)\n" \
  "v_mfma_f32_16x16x32_bf16 v[224:227], v[84:87], v[108:111], v[224:227]\n" \
  "s_setprio 2\n" \
  "ds_read_b64_tr_b16 v[100:101], v140 offset:7168\n" \
  "v_mfma_f32_16x16x32_bf16 v[228:231], v[84:87], v[112:115], v[228:231]\n" \
  "ds_read_b64_tr_b16 v[102:103], v140 offset:10752\n" \
  "v_mfma_f32_16x16x32_bf16 v[232:235], v[88:91], v[108:111], v[232:235]\n" \
  "ds_read_b64_tr_b16 v[104:105], v140 offset:21504\n" \
  "v_mfma_f32_16x16x32_bf16 v[236:239], v[88:91], v[112:115], v[236:239]\n" \
  "ds_read_b64_tr_b16 v[106:107], v140 offset:25088\n" \
  "s_waitcnt lgkmcnt(4)\n" \
  "v_mfma_f32_16x16x32_bf16 v[240:243], v[92:95], v[108:111], v[240:243]\n" \
  "ds_read_b64_tr_b16 v[76:77], v140 offset:7200\n" \
  "v_mfma_f32_16x16x32_bf16 v[244:247], v[92:95], v[112:115], v[244:247]\n" \
  "ds_read_b64_tr_b16 v[78:79], v140 offset:10784\n" \
  "v_mfma_f32_16x16x32_bf16 v[248:251], v[96:99], v[108:111], v[248:251]\n" \
  "ds_read_b64_tr_b16 v[80:81], v140 offset:21536\n" \
  "v_mfma_f32_16x16x32_bf16 v[252:255], v[96:99], v[112:115], v[252:255]\n" \
  "ds_read_b64_tr_b16 v[82:83], v140 offset:25120\n" \
  "s_waitcnt vmcnt(7) lgkmcnt(4)\n" \
  "v_mfma_f32_16x16x32_bf16 v[144:147], v[100:103], v[116:119], v[144:147]\n" \
  "ds_read_b64_tr_b16 v[84:85], v140 offset:7232\n" \
  "v_mfma_f32_16x16x32_bf16 v[148:151], v[100:103], v[120:123], v[148:151]\n" \
  "ds_read_b64_tr_b16 v[86:87], v140 offset:10816\n" \
  "s_add_u32 m0, s51, 0\n" \
  "s_nop 0\n" \
  "buffer_load_dwordx4 %15, s[44:47], s49 offen lds\n" \
  "v_mfma_f32_16x16x32_bf16 v[152:155], v[104:107], v[116:119], v[152:155]\n" \
  "ds_read_b64_tr_b16 v[88:89], v140 offset:21568\n" \
  "v_mfma_f32_16x16x32_bf16 v[156:159], v[104:107], v[120:123], v[156:159]\n" \
  "ds_read_b64_tr_b16 v[90:91], v140 offset:25152\n" \
  "s_waitcnt lgkmcnt(4)\n" \
  "v_mfma_f32_16x16x32_bf16 v[160:163], v[76:79], v[116:119], v[160:163]\n" \
  "ds_read_b64_tr_b16 v[92:93], v140 offset:7264\n" \
  "v_mfma_f32_16x16x32_bf16 v[164:167], v[76:79], v[120:123], v[164:167]\n" \
  "ds_read_b64_tr_b16 v[94:95], v140 offset:10848\n" \
  "v_mfma_f32_16x16x32_bf16 v[168:171], v[80:83], v[116:119], v[168:171]\n" \
  "ds_read_b64_tr_b16 v[96:97], v140 offset:21600\n" \
  "v_mfma_f32_16x16x32_bf16 v[172:175], v[80:83], v[120:123], v[172:175]\n" \
  "ds_read_b64_tr_b16 v[98:99], v140 offset:25184\n" \
  "s_waitcnt lgkmcnt(4)\n" \
  "v_mfma_f32_16x16x32_bf16 v[176:179], v[84:87], v[116:119], v[176:179]\n" \
  "ds_read_b64_tr_b16 v[100:101], v140 offset:7296\n" \
  "v_mfma_f32_16x16x32_bf16 v[180:183], v[84:87], v[120:123], v[180:183]\n" \
  "ds_read_b64_tr_b16 v[102:103], v140 offset:10880\n" \
  "s_add_u32 m0, s51, 4096\n" \
  "s_nop 0\n" \
  "buffer_load_dwordx4 %16, s[44:47], s49 offen lds\n" \
  "v_mfma_f32_16x16x32_bf16 v[184:187], v[88:91], v[116:119], v[184:187]\n" \
  "ds_read_b64_tr_b16 v[104:105], v140 offset:21632\n" \
  "v_mfma_f32_16x16x32_bf16 v[188:191], v[88:91], v[120:123], v[188:191]\n" \
  "ds_read_b64_tr_b16 v[106:107], v140 offset:25216\n" \
  "s_waitcnt lgkmcnt(4)\n" \
  "v_mfma_f32_16x16x32_bf16 v[192:195], v[92:95], v[116:119], v[192:195]\n" \
  "ds_read_b64_tr_b16 v[76:77], v140 offset:7328\n" \
  "v_mfma_f32_16x16x32_bf16 v[196:199], v[92:95], v[120:123], v[196:199]\n" \
  "ds_read_b64_tr_b16 v[78:79], v140 offset:10912\n" \
  "buffer_load_dwordx4 v[108:111], %13, s[40:43], s48 offen offset:0 sc1\n" \
  "v_mfma_f32_16x16x32_bf16 v[200:203], v[96:99], v[116:119], v[200:203]\n" \
  "ds_read_b64_tr_b16 v[80:81], v140 offset:21664\n" \
  "v_mfma_f32_16x16x32_bf16 v[204:207], v[96:99], v[120:123], v[204:207]\n" \
  "ds_read_b64_tr_b16 v[82:83], v140 offset:25248\n" \
  "buffer_load_dwordx4 v[112:115], %13, s[40:43], s48 offen offset:1024 sc1\n" \
  "s_waitcnt lgkmcnt(4)\n" \
  "v_mfma_f32_16x16x32_bf16 v[208:211], v[100:103], v[116:119], v[208:211]\n" \
  "ds_read_b64_tr_b16 v[84:85], v140 offset:7360\n" \
  "v_mfma_f32_16x16x32_bf16 v[212:215], v[100:103], v[120:123], v[212:215]\n" \
  "ds_read_b64_tr_b16 v[86:87], v140 offset:10944\n" \
  "v_mfma_f32_16x16x32_bf16 v[216:219], v[104:107], v[116:119], v[216:219]\n" \
  "ds_read_b64_tr_b16 v[88:89], v140 offset:21696\n" \
  "v_mfma_f32_16x16x32_bf16 v[220:223], v[104:107], v[120:123], v[220:223]\n" \
  "ds_read_b64_tr_b16 v[90:91], v140 offset:25280\n" \
  "s_add_u32 m0, s51, 8192\n" \
  "s_nop 0\n" \
  "buffer_load_dwordx4 %17, s[44:47], s49 offen lds\n" \
  "s_waitcnt lgkmcnt(4)\n" \
  "v_mfma_f32_16x16x32_bf16 v[224:227], v[76:79], v[116:119], v[224:227]\n" \
  "s_add_u32 s50, s50, 28672\n" \
  "s_cmp_eq_u32 s50, 143360\n" \
  "s_cselect_b32 s50, 0, s50\n" \
  "v_add_u32 v140, s50, %14\n" \
  "s_setprio 1\n" \
  "ds_read_b64_tr_b16 v[92:93], v140 offset:0\n" \
  "v_mfma_f32_16x16x32_bf16 v[228:231], v[76:79], v[120:123], v[228:231]\n" \
  "ds_read_b64_tr_b16 v[94:95], v140 offset:3584\n" \
  "v_mfma_f32_16x16x32_bf16 v[232:235], v[80:83], v[116:119], v[232:235]\n" \
  "ds_read_b64_tr_b16 v[96:97], v140 offset:14336\n" \
  "v_mfma_f32_16x16x32_bf16 v[236:239], v[80:83], v[120:123], v[236:239]\n" \
  "ds_read_b64_tr_b16 v[98:99], v140 offset:17920\n" \
  "s_waitcnt lgkmcnt(4)\n" \
  "v_mfma_f32_16x16x32_bf16 v[240:243], v[84:87], v[116:119], v[240:243]\n" \
  "ds_read_b64_tr_b16 v[100:101], v140 offset:32\n" \
  "v_mfma_f32_16x16x32_bf16 v[244:247], v[84:87], v[120:123], v[244:247]\n" \
  "ds_read_b64_tr_b16 v[102:103], v140 offset:3616\n" \
  "v_mfma_f32_16x16x32_bf16 v[248:251], v[88:91], v[116:119], v[248:251]\n" \
  "ds_read_b64_tr_b16 v[104:105], v140 offset:14368\n" \
  "v_mfma_f32_16x16x32_bf16 v[252:255], v[88:91], v[120:123], v[252:255]\n" \
  "ds_read_b64_tr_b16 v[106:107], v140 offset:17952\n" \
  "s_waitcnt vmcnt(10) lgkmcnt(4)\n" \
  "v_mfma_f32_16x16x32_bf16 v[144:147], v[92:95], v[124:127], v[144:147]\n" \
  "ds_read_b64_tr_b16 v[76:77], v140 offset:64\n" \
  "v_mfma_f32_16x16x32_bf16 v[148:151], v[92:95], v[128:131], v[148:151]\n" \
  "ds_read_b64_tr_b16 v[78:79], v140 offset:3648\n" \
  "s_cmp_eq_u32 %12, 0\n" \
  "s_cbranch_scc1 Lskip6_%=\n" \
  "s_add_u32 m0, s51, 12288\n" \
  "s_nop 0\n" \
  "buffer_load_dwordx4 %18, s[44:47], s49 offen lds\n" \
  "Lskip6_%=:\n" \
  "s_add_u32 s49, s49, 458752\n" \
  "s_add_u32 s51, s51, 28672\n" \
  "s_sub_u32 s53, s51, %11\n" \
  "s_cmp_eq_u32 s53, 143360\n" \
  "s_cselect_b32 s51, %11, s51\n" \
  "v_mfma_f32_16x16x32_bf16 v[152:155], v[96:99], v[124:127], v[152:155]\n" \
  "ds_read_b64_tr_b16 v[80:81], v140 offset:14400\n" \
  "v_mfma_f32_16x16x32_bf16 v[156:159], v[96:99], v[128:131], v[156:159]\n" \
  "ds_read_b64_tr_b16 v[82:83], v140 offset:17984\n" \
  "s_waitcnt lgkmcnt(4)\n" \
  "v_mfma_f32_16x16x32_bf16 v[160:163], v[100:103], v[124:127], v[160:163]\n" \
  "ds_read_b64_tr_b16 v[84:85], v140 offset:96\n" \
  "v_mfma_f32_16x16x32_bf16 v[164:167], v[100:103], v[128:131], v[164:167]\n" \
  "ds_read_b64_tr_b16 v[86:87], v140 offset:3680\n" \
  "v_mfma_f32_16x16x32_bf16 v[168:171], v[104:107], v[124:127], v[168:171]\n" \
  "ds_read_b64_tr_b16 v[88:89], v140 offset:14432\n" \
  "v_mfma_f32_16x16x32_bf16 v[172:175], v[104:107], v[128:131], v[172:175]\n" \
  "ds_read_b64_tr_b16 v[90:91], v140 offset:18016\n" \
  "s_waitcnt lgkmcnt(4)\n" \
  "v_mfma_f32_16x16x32_bf16 v[176:179], v[76:79], v[124:127], v[176:179]\n" \
  "ds_read_b64_tr_b16 v[92:93], v140 offset:128\n" \
  "v_mfma_f32_16x16x32_bf16 v[180:183], v[76:79], v[128:131], v[180:183]\n" \
  "ds_read_b64_tr_b16 v[94:95], v140 offset:3712\n" \
  "v_mfma_f32_16x16x32_bf16 v[184:187], v[80:83], v[124:127], v[184:187]\n" \
  "ds_read_b64_tr_b16 v[96:97], v140 offset:14464\n" \
  "v_mfma_f32_16x16x32_bf16 v[188:191], v[80:83], v[128:131], v[188:191]\n" \
  "ds_read_b64_tr_b16 v[98:99], v140 offset:18048\n" \
  "s_waitcnt lgkmcnt(4)\n" \
  "v_mfma_f32_16x16x32_bf16 v[192:195], v[84:87], v[124:127], v[192:195]\n" \
  "ds_read_b64_tr_b16 v[100:101], v140 offset:160\n" \
  "v_mfma_f32_16x16x32_bf16 v[196:199], v[84:87], v[128:131], v[196:199]\n" \
  "ds_read_b64_tr_b16 v[102:103], v140 offset:3744\n" \
  "buffer_load_dwordx4 v[116:119], %13, s[40:43], s48 offen offset:2048 sc1\n" \
  "v_mfma_f32_16x16x32_bf16 v[200:203], v[88:91], v[124:127], v[200:203]\n" \
  "ds_read_b64_tr_b16 v[104:105], v140 offset:14496\n" \
  "v_mfma_f32_16x16x32_bf16 v[204:207], v[88:91], v[128:131], v[204:207]\n" \
  "ds_read_b64_tr_b16 v[106:107], v140 offset:18080\n" \
  "buffer_load_dwordx4 v[120:123], %13, s[40:43], s48 offen offset:3072 sc1\n" \
  "s_add_u32 s48, s48, 4096\n" \
  "s_waitcnt lgkmcnt(4)\n" \
  "v_mfma_f32_16x16x32_bf16 v[208:211], v[92:95], v[124:127], v[208:211]\n" \
  "ds_read_b64_tr_b16 v[76:77], v140 offset:192\n" \
  "v_mfma_f32_16x16x32_bf16 v[212:215], v[92:95], v[128:131], v[212:215]\n" \
  "ds_read_b64_tr_b16 v[78:79], v140 offset:3776\n" \
  "v_mfma_f32_16x16x32_bf16 v[216:219], v[96:99], v[124:127], v[216:219]\n" \
  "ds_read_b64_tr_b16 v[80:81], v140 offset:14528\n" \
  "v_mfma_f32_16x16x32_bf16 v[220:223], v[96:99], v[128:131], v[220:223]\n" \
  "ds_read_b64_tr_b16 v[82:83], v140 offset:18112\n" \
  "s_waitcnt lgkmcnt(4)\n" \
  "v_mfma_f32_16x16x32_bf16 v[224:227], v[100:103], v[124:127], v[224:227]\n" \
  "s_setprio 0\n" \
  "ds_read_b64_tr_b16 v[84:85], v140 offset:7168\n" \
  "v_mfma_f32_16x16x32_bf16 v[228:231], v[100:103], v[128:131], v[228:231]\n" \
  "ds_read_b64_tr_b16 v[86:87], v140 offset:10752\n" \
  "v_mfma_f32_16x16x32_bf16 v[232:235], v[104:107], v[124:127], v[232:235]\n" \
  "ds_read_b64_tr_b16 v[88:89], v140 offset:21504\n" \
  "v_mfma_f32_16x16x32_bf16 v[236:239], v[104:107], v[128:131], v[236:239]\n" \
  "ds_read_b64_tr_b16 v[90:91], v140 offset:25088\n" \
  "s_waitcnt lgkmcnt(4)\n" \
  "v_mfma_f32_16x16x32_bf16 v[240:243], v[76:79], v[124:127], v[240:243]\n" \
  "ds_read_b64_tr_b16 v[92:93], v140 offset:7200\n" \
  "v_mfma_f32_16x16x32_bf16 v[244:247], v[76:79], v[128:131], v[244:247]\n" \
  "ds_read_b64_tr_b16 v[94:95], v140 offset:10784\n" \
  "v_mfma_f32_16x16x32_bf16 v[248:251], v[80:83], v[124:127], v[248:251]\n" \
  "ds_read_b64_tr_b16 v[96:97], v140 offset:21536\n" \
  "v_mfma_f32_16x16x32_bf16 v[252:255], v[80:83], v[128:131], v[252:255]\n" \
  "ds_read_b64_tr_b16 v[98:99], v140 offset:25120\n" \
  "s_waitcnt vmcnt(7) lgkmcnt(4)\n" \
  "v_mfma_f32_16x16x32_bf16 v[144:147], v[84:87], v[132:135], v[144:147]\n" \
  "ds_read_b64_tr_b16 v[100:101], v140 offset:7232\n" \
  "v_mfma_f32_16x16x32_bf16 v[148:151], v[84:87], v[136:139], v[148:151]\n" \
  "ds_read_b64_tr_b16 v[102:103], v140 offset:10816\n" \
  "v_mfma_f32_16x16x32_bf16 v[152:155], v[88:91], v[132:135], v[152:155]\n" \
  "ds_read_b64_tr_b16 v[104:105], v140 offset:21568\n" \
  "v_mfma_f32_16x16x32_bf16 v[156:159], v[88:91], v[136:139], v[156:159]\n" \
  "ds_read_b64_tr_b16 v[106:107], v140 offset:25152\n" \
  "s_waitcnt lgkmcnt(4)\n" \
  "v_mfma_f32_16x16x32_bf16 v[160:163], v[92:95], v[132:135], v[160:163]\n" \
  "ds_read_b64_tr_b16 v[76:77], v140 offset:7264\n" \
  "v_mfma_f32_16x16x32_bf16 v[164:167], v[92:95], v[136:139], v[164:167]\n" \
  "ds_read_b64_tr_b16 v[78:79], v140 offset:10848\n" \
  "v_mfma_f32_16x16x32_bf16 v[168:171], v[96:99], v[132:135], v[168:171]\n" \
  "ds_read_b64_tr_b16 v[80:81], v140 offset:21600\n" \
  "v_mfma_f32_16x16x32_bf16 v[172:175], v[96:99], v[136:139], v[172:175]\n" \
  "ds_read_b64_tr_b16 v[82:83], v140 offset:25184\n" \
  "s_waitcnt lgkmcnt(4)\n" \
  "v_mfma_f32_16x16x32_bf16 v[176:179], v[100:103], v[132:135], v[176:179]\n" \
  "ds_read_b64_tr_b16 v[84:85], v140 offset:7296\n" \
  "v_mfma_f32_16x16x32_bf16 v[180:183], v[100:103], v[136:139], v[180:183]\n" \
  "ds_read_b64_tr_b16 v[86:87], v140 offset:10880\n" \
  "v_mfma_f32_16x16x32_bf16 v[184:187], v[104:107], v[132:135], v[184:187]\n" \
  "ds_read_b64_tr_b16 v[88:89], v140 offset:21632\n" \
  "v_mfma_f32_16x16x32_bf16 v[188:191], v[104:107], v[136:139], v[188:191]\n" \
  "ds_read_b64_tr_b16 v[90:91], v140 offset:25216\n" \
  "s_waitcnt lgkmcnt(4)\n" \
  "v_mfma_f32_16x16x32_bf16 v[192:195], v[76:79], v[132:135], v[192:195]\n" \
  "ds_read_b64_tr_b16 v[92:93], v140 offset:7328\n" \
  "v_mfma_f32_16x16x32_bf16 v[196:199], v[76:79], v[136:139], v[196:199]\n" \
  "ds_read_b64_tr_b16 v[94:95], v140 offset:10912\n" \
  "buffer_load_dwordx4 v[124:127], %13, s[40:43], s48 offen offset:0 sc1\n" \
  "v_mfma_f32_16x16x32_bf16 v[200:203], v[80:83], v[132:135], v[200:203]\n" \
  "ds_read_b64_tr_b16 v[96:97], v140 offset:21664\n" \
  "v_mfma_f32_16x16x32_bf16 v[204:207], v[80:83], v[136:139], v[204:207]\n" \
  "ds_read_b64_tr_b16 v[98:99], v140 offset:25248\n" \
  "buffer_load_dwordx4 v[128:131], %13, s[40:43], s48 offen offset:1024 sc1\n" \
  "s_waitcnt lgkmcnt(4)\n" \
  "v_mfma_f32_16x16x32_bf16 v[208:211], v[84:87], v[132:135], v[208:211]\n" \
  "ds_read_b64_tr_b16 v[100:101], v140 offset:7360\n" \
  "v_mfma_f32_16x16x32_bf16 v[212:215], v[84:87], v[136:139], v[212:215]\n" \
  "ds_read_b64_tr_b16 v[102:103], v140 offset:10944\n" \
  "v_mfma_f32_16x16x32_bf16 v[216:219], v[88:91], v[132:135], v[216:219]\n" \
  "ds_read_b64_tr_b16 v[104:105], v140 offset:21696\n" \
  "v_mfma_f32_16x16x32_bf16 v[220:223], v[88:91], v[136:139], v[220:223]\n" \
  "ds_read_b64_tr_b16 v[106:107], v140 offset:25280\n" \
  "s_waitcnt lgkmcnt(4)\n" \
  "v_mfma_f32_16x16x32_bf16 v[224:227], v[92:95], v[132:135], v[224:227]\n" \
  "s_add_u32 s50, s50, 28672\n" \
  "s_cmp_eq_u32 s50, 143360\n" \
  "s_cselect_b32 s50, 0, s50\n" \
  "v_add_u32 v140, s50, %14\n" \
  "s_waitcnt vmcnt(4)\n" \
  "s_barrier\n" \
  "s_setprio 3\n" \
  "ds_read_b64_tr_b16 v[76:77], v140 offset:0\n" \
  "v_mfma_f32_16x16x32_bf16 v[228:231], v[92:95], v[136:139], v[228:231]\n" \
  "ds_read_b64_tr_b16 v[78:79], v140 offset:3584\n" \
  "s_add_u32 m0, s51, 0\n" \
  "s_nop 0\n" \
  "buffer_load_dwordx4 %15, s[44:47], s49 offen lds\n" \
  "v_mfma_f32_16x16x32_bf16 v[232:235], v[96:99], v[132:135], v[232:235]\n" \
  "ds_read_b64_tr_b16 v[80:81], v140 offset:14336\n" \
  "v_mfma_f32_16x16x32_bf16 v[236:239], v[96:99], v[136:139], v[236:239]\n" \
  "ds_read_b64_tr_b16 v[82:83], v140 offset:17920\n" \
  "s_waitcnt lgkmcnt(4)\n" \
  "v_mfma_f32_16x16x32_bf16 v[240:243], v[100:103], v[132:135], v[240:243]\n" \
  "ds_read_b64_tr_b16 v[84:85], v140 offset:32\n" \
  "v_mfma_f32_16x16x32_bf16 v[244:247], v[100:103], v[136:139], v[244:247]\n" \
  "ds_read_b64_tr_b16 v[86:87], v140 offset:3616\n" \
  "v_mfma_f32_16x16x32_bf16 v[248:251], v[104:107], v[132:135], v[248:251]\n" \
  "ds_read_b64_tr_b16 v[88:89], v140 offset:14368\n" \
  "v_mfma_f32_16x16x32_bf16 v[252:255], v[104:107], v[136:139], v[252:255]\n" \
  "ds_read_b64_tr_b16 v[90:91], v140 offset:17952\n" \
  "s_sub_u32 s52, s52, 1\n" \
  "s_cmp_lg_u32 s52, 0\n" \
  "s_cbranch_scc1 Lloop_%=\n" \
  "s_waitcnt vmcnt(6) lgkmcnt(4)\n" \
  "v_mfma_f32_16x16x32_bf16 v[144:147], v[76:79], v[108:111], v[144:147]\n" \
  "ds_read_b64_tr_b16 v[92:93], v140 offset:64\n" \
  "v_mfma_f32_16x16x32_bf16 v[148:151], v[76:79], v[112:115], v[148:151]\n" \
  "ds_read_b64_tr_b16 v[94:95], v140 offset:3648\n" \
  "s_add_u32 m0, %11, 118784\n" \
  "s_mov_b32 s53, 6422528\n" \
  "buffer_load_dwordx4 %16, s[44:47], s53 offen lds\n" \
  "v_mfma_f32_16x16x32_bf16 v[152:155], v[80:83], v[108:111], v[152:155]\n" \
  "ds_read_b64_tr_b16 v[96:97], v140 offset:14400\n" \
  "v_mfma_f32_16x16x32_bf16 v[156:159], v[80:83], v[112:115], v[156:159]\n" \
  "ds_read_b64_tr_b16 v[98:99], v140 offset:17984\n" \
  "s_waitcnt lgkmcnt(4)\n" \
  "v_mfma_f32_16x16x32_bf16 v[160:163], v[84:87], v[108:111], v[160:163]\n" \
  "ds_read_b64_tr_b16 v[100:101], v140 offset:96\n" \
  "v_mfma_f32_16x16x32_bf16 v[164:167], v[84:87], v[112:115], v[164:167]\n" \
  "ds_read_b64_tr_b16 v[102:103], v140 offset:3680\n" \
  "v_mfma_f32_16x16x32_bf16 v[168:171], v[88:91], v[108:111], v[168:171]\n" \
  "ds_read_b64_tr_b16 v[104:105], v140 offset:14432\n" \
  "v_mfma_f32_16x16x32_bf16 v[172:175], v[88:91], v[112:115], v[172:175]\n" \
  "ds_read_b64_tr_b16 v[106:107], v140 offset:18016\n" \
  "s_waitcnt lgkmcnt(4)\n" \
  "v_mfma_f32_16x16x32_bf16 v[176:179], v[92:95], v[108:111], v[176:179]\n" \
  "ds_read_b64_tr_b16 v[76:77], v140 offset:128\n" \
  "v_mfma_f32_16x16x32_bf16 v[180:183], v[92:95], v[112:115], v[180:183]\n" \
  "ds_read_b64_tr_b16 v[78:79], v140 offset:3712\n" \
  "v_mfma_f32_16x16x32_bf16 v[184:187], v[96:99], v[108:111], v[184:187]\n" \
  "ds_read_b64_tr_b16 v[80:81], v140 offset:14464\n" \
  "v_mfma_f32_16x16x32_bf16 v[188:191], v[96:99], v[112:115], v[188:191]\n" \
  "ds_read_b64_tr_b16 v[82:83], v140 offset:18048\n" \
  "s_add_u32 m0, %11, 122880\n" \
  "s_mov_b32 s53, 6422528\n" \
  "buffer_load_dwordx4 %17, s[44:47], s53 offen lds\n" \
  "s_waitcnt lgkmcnt(4)\n" \
  "v_mfma_f32_16x16x32_bf16 v[192:195], v[100:103], v[108:111], v[192:195]\n" \
  "ds_read_b64_tr_b16 v[84:85], v140 offset:160\n" \
  "v_mfma_f32_16x16x32_bf16 v[196:199], v[100:103], v[112:115], v[196:199]\n" \
  "ds_read_b64_tr_b16 v[86:87], v140 offset:3744\n" \
  "s_mov_b32 s53, 53248\n" \
  "buffer_load_dwordx4 v[132:135], %13, s[40:43], s53 offen offset:2048 sc1\n" \
  "v_mfma_f32_16x16x32_bf16 v[200:203], v[104:107], v[108:111], v[200:203]\n" \
  "ds_read_b64_tr_b16 v[88:89], v140 offset:14496\n" \
  "v_mfma_f32_16x16x32_bf16 v[204:207], v[104:107], v[112:115], v[204:207]\n" \
  "ds_read_b64_tr_b16 v[90:91], v140 offset:18080\n" \
  "s_mov_b32 s53, 53248\n" \
  "buffer_load_dwordx4 v[136:139], %13, s[40:43], s53 offen offset:3072 sc1\n" \
  "s_waitcnt lgkmcnt(4)\n" \
  "v_mfma_f32_16x16x32_bf16 v[208:211], v[76:79], v[108:111], v[208:211]\n" \
  "ds_read_b64_tr_b16 v[92:93], v140 offset:192\n" \
  "v_mfma_f32_16x16x32_bf16 v[212:215], v[76:79], v[112:115], v[212:215]\n" \
  "ds_read_b64_tr_b16 v[94:95], v140 offset:3776\n" \
  "v_mfma_f32_16x16x32_bf16 v[216:219], v[80:83], v[108:111], v[216:219]\n" \
  "ds_read_b64_tr_b16 v[96:97], v140 offset:14528\n" \
  "v_mfma_f32_16x16x32_bf16 v[220:223], v[80:83], v[112:115], v[220:223]\n" \
  "ds_read_b64_tr_b16 v[98:99], v140 offset:18112\n" \
  "s_cmp_eq_u32 %12, 0\n" \
  "s_cbranch_scc1 Lskip7_%=\n" \
  "s_add_u32 m0, %11, 126976\n" \
  "s_mov_b32 s53, 6422528\n" \
  "buffer_load_dwordx4 %18, s[44:47], s53 offen lds\n" \
  "Lskip7_%=:\n" \
  "s_waitcnt lgkmcnt(4)\n" \
  "v_mfma_f32_16x16x32_bf16 v[224:227], v[84:87], v[108:111], v[224:227]\n" \
  "s_setprio 2\n" \
  "ds_read_b64_tr_b16 v[100:101], v140 offset:7168\n" \
  "v_mfma_f32_16x16x32_bf16 v[228:231], v[84:87], v[112:115], v[228:231]\n" \
  "ds_read_b64_tr_b16 v[102:103], v140 offset:10752\n" \
  "v_mfma_f32_16x16x32_bf16 v[232:235], v[88:91], v[108:111], v[232:235]\n" \
  "ds_read_b64_tr_b16 v[104:105], v140 offset:21504\n" \
  "v_mfma_f32_16x16x32_bf16 v[236:239], v[88:91], v[112:115], v[236:239]\n" \
  "ds_read_b64_tr_b16 v[106:107], v140 offset:25088\n" \
  "s_waitcnt lgkmcnt(4)\n" \
  "v_mfma_f32_16x16x32_bf16 v[240:243], v[92:95], v[108:111], v[240:243]\n" \
  "ds_read_b64_tr_b16 v[76:77], v140 offset:7200\n" \
  "v_mfma_f32_16x16x32_bf16 v[244:247], v[92:95], v[112:115], v[244:247]\n" \
  "ds_read_b64_tr_b16 v[78:79], v140 offset:10784\n" \
  "v_mfma_f32_16x16x32_bf16 v[248:251], v[96:99], v[108:111], v[248:251]\n" \
  "ds_read_b64_tr_b16 v[80:81], v140 offset:21536\n" \
  "v_mfma_f32_16x16x32_bf16 v[252:255], v[96:99], v[112:115], v[252:255]\n" \
  "ds_read_b64_tr_b16 v[82:83], v140 offset:25120\n" \
  "s_waitcnt vmcnt(7) lgkmcnt(4)\n" \
  "v_mfma_f32_16x16x32_bf16 v[144:147], v[100:103], v[116:119], v[144:147]\n" \
  "ds_read_b64_tr_b16 v[84:85], v140 offset:7232\n" \
  "v_mfma_f32_16x16x32_bf16 v[148:151], v[100:103], v[120:123], v[148:151]\n" \
  "ds_read_b64_tr_b16 v[86:87], v140 offset:10816\n" \
  "s_add_u32 m0, %11, 0\n" \
  "s_mov_b32 s53, 6881280\n" \
  "buffer_load_dwordx4 %15, s[44:47], s53 offen lds\n" \
  "v_mfma_f32_16x16x32_bf16 v[152:155], v[104:107], v[116:119], v[152:155]\n" \
  "ds_read_b64_tr_b16 v[88:89], v140 offset:21568\n" \
  "v_mfma_f32_16x16x32_bf16 v[156:159], v[104:107], v[120:123], v[156:159]\n" \
  "ds_read_b64_tr_b16 v[90:91], v140 offset:25152\n" \
  "s_waitcnt lgkmcnt(4)\n" \
  "v_mfma_f32_16x16x32_bf16 v[160:163], v[76:79], v[116:119], v[160:163]\n" \
  "ds_read_b64_tr_b16 v[92:93], v140 offset:7264\n" \
  "v_mfma_f32_16x16x32_bf16 v[164:167], v[76:79], v[120:123], v[164:167]\n" \
  "ds_read_b64_tr_b16 v[94:95], v140 offset:10848\n" \
  "v_mfma_f32_16x16x32_bf16 v[168:171], v[80:83], v[116:119], v[168:171]\n" \
  "ds_read_b64_tr_b16 v[96:97], v140 offset:21600\n" \
  "v_mfma_f32_16x16x32_bf16 v[172:175], v[80:83], v[120:123], v[172:175]\n" \
  "ds_read_b64_tr_b16 v[98:99], v140 offset:25184\n" \
  "s_waitcnt lgkmcnt(4)\n" \
  "v_mfma_f32_16x16x32_bf16 v[176:179], v[84:87], v[116:119], v[176:179]\n" \
  "ds_read_b64_tr_b16 v[100:101], v140 offset:7296\n" \
  "v_mfma_f32_16x16x32_bf16 v[180:183], v[84:87], v[120:123], v[180:183]\n" \
  "ds_read_b64_tr_b16 v[102:103], v140 offset:10880\n" \
  "s_add_u32 m0, %11, 4096\n" \
  "s_mov_b32 s53, 6881280\n" \
  "buffer_load_dwordx4 %16, s[44:47], s53 offen lds\n" \
  "v_mfma_f32_16x16x32_bf16 v[184:187], v[88:91], v[116:119], v[184:187]\n" \
  "ds_read_b64_tr_b16 v[104:105], v140 offset:21632\n" \
  "v_mfma_f32_16x16x32_bf16 v[188:191], v[88:91], v[120:123], v[188:191]\n" \
  "ds_read_b64_tr_b16 v[106:107], v140 offset:25216\n" \
  "s_waitcnt lgkmcnt(4)\n" \
  "v_mfma_f32_16x16x32_bf16 v[192:195], v[92:95], v[116:119], v[192:195]\n" \
  "ds_read_b64_tr_b16 v[76:77], v140 offset:7328\n" \
  "v_mfma_f32_16x16x32_bf16 v[196:199], v[92:95], v[120:123], v[196:199]\n" \
  "ds_read_b64_tr_b16 v[78:79], v140 offset:10912\n" \
  "s_mov_b32 s53, 57344\n" \
  "buffer_load_dwordx4 v[108:111], %13, s[40:43], s53 offen offset:0 sc1\n" \
  "v_mfma_f32_16x16x32_bf16 v[200:203], v[96:99], v[116:119], v[200:203]\n" \
  "ds_read_b64_tr_b16 v[80:81], v140 offset:21664\n" \
  "v_mfma_f32_16x16x32_bf16 v[204:207], v[96:99], v[120:123], v[204:207]\n" \
  "ds_read_b64_tr_b16 v[82:83], v140 offset:25248\n" \
  "s_mov_b32 s53, 57344\n" \
  "buffer_load_dwordx4 v[112:115], %13, s[40:43], s53 offen offset:1024 sc1\n" \
  "s_waitcnt lgkmcnt(4)\n" \
  "v_mfma_f32_16x16x32_bf16 v[208:211], v[100:103], v[116:119], v[208:211]\n" \
  "ds_read_b64_tr_b16 v[84:85], v140 offset:7360\n" \
  "v_mfma_f32_16x16x32_bf16 v[212:215], v[100:103], v[120:123], v[212:215]\n" \
  "ds_read_b64_tr_b16 v[86:87], v140 offset:10944\n" \
  "v_mfma_f32_16x16x32_bf16 v[216:219], v[104:107], v[116:119], v[216:219]\n" \
  "ds_read_b64_tr_b16 v[88:89], v140 offset:21696\n" \
  "v_mfma_f32_16x16x32_bf16 v[220:223], v[104:107], v[120:123], v[220:223]\n" \
  "ds_read_b64_tr_b16 v[90:91], v140 offset:25280\n" \
  "s_add_u32 m0, %11, 8192\n" \
  "s_mov_b32 s53, 6881280\n" \
  "buffer_load_dwordx4 %17, s[44:47], s53 offen lds\n" \
  "s_waitcnt lgkmcnt(4)\n" \
  "v_mfma_f32_16x16x32_bf16 v[224:227], v[76:79], v[116:119], v[224:227]\n" \
  "v_add_u32 v140, 86016, %14\n" \
  "s_setprio 1\n" \
  "ds_read_b64_tr_b16 v[92:93], v140 offset:0\n" \
  "v_mfma_f32_16x16x32_bf16 v[228:231], v[76:79], v[120:123], v[228:231]\n" \
  "ds_read_b64_tr_b16 v[94:95], v140 offset:3584\n" \
  "v_mfma_f32_16x16x32_bf16 v[232:235], v[80:83], v[116:119], v[232:235]\n" \
  "ds_read_b64_tr_b16 v[96:97], v140 offset:14336\n" \
  "v_mfma_f32_16x16x32_bf16 v[236:239], v[80:83], v[120:123], v[236:239]\n" \
  "ds_read_b64_tr_b16 v[98:99], v140 offset:17920\n" \
  "s_waitcnt lgkmcnt(4)\n" \
  "v_mfma_f32_16x16x32_bf16 v[240:243], v[84:87], v[116:119], v[240:243]\n" \
  "ds_read_b64_tr_b16 v[100:101], v140 offset:32\n" \
  "v_mfma_f32_16x16x32_bf16 v[244:247], v[84:87], v[120:123], v[244:247]\n" \
  "ds_read_b64_tr_b16 v[102:103], v140 offset:3616\n" \
  "v_mfma_f32_16x16x32_bf16 v[248:251], v[88:91], v[116:119], v[248:251]\n" \
  "ds_read_b64_tr_b16 v[104:105], v140 offset:14368\n" \
  "v_mfma_f32_16x16x32_bf16 v[252:255], v[88:91], v[120:123], v[252:255]\n" \
  "ds_read_b64_tr_b16 v[106:107], v140 offset:17952\n" \
  "s_waitcnt vmcnt(10) lgkmcnt(4)\n" \
  "v_mfma_f32_16x16x32_bf16 v[144:147], v[92:95], v[124:127], v[144:147]\n" \
  "ds_read_b64_tr_b16 v[76:77], v140 offset:64\n" \
  "v_mfma_f32_16x16x32_bf16 v[148:151], v[92:95], v[128:131], v[148:151]\n" \
  "ds_read_b64_tr_b16 v[78:79], v140 offset:3648\n" \
  "s_cmp_eq_u32 %12, 0\n" \
  "s_cbranch_scc1 Lskip8_%=\n" \
  "s_add_u32 m0, %11, 12288\n" \
  "s_mov_b32 s53, 6881280\n" \
  "buffer_load_dwordx4 %18, s[44:47], s53 offen lds\n" \
  "Lskip8_%=:\n" \
  "v_mfma_f32_16x16x32_bf16 v[152:155], v[96:99], v[124:127], v[152:155]\n" \
  "ds_read_b64_tr_b16 v[80:81], v140 offset:14400\n" \
  "v_mfma_f32_16x16x32_bf16 v[156:159], v[96:99], v[128:131], v[156:159]\n" \
  "ds_read_b64_tr_b16 v[82:83], v140 offset:17984\n" \
  "s_waitcnt lgkmcnt(4)\n" \
  "v_mfma_f32_16x16x32_bf16 v[160:163], v[100:103], v[124:127], v[160:163]\n" \
  "ds_read_b64_tr_b16 v[84:85], v140 offset:96\n" \
  "v_mfma_f32_16x16x32_bf16 v[164:167], v[100:103], v[128:131], v[164:167]\n" \
  "ds_read_b64_tr_b16 v[86:87], v140 offset:3680\n" \
  "v_mfma_f32_16x16x32_bf16 v[168:171], v[104:107], v[124:127], v[168:171]\n" \
  "ds_read_b64_tr_b16 v[88:89], v140 offset:14432\n" \
  "v_mfma_f32_16x16x32_bf16 v[172:175], v[104:107], v[128:131], v[172:175]\n" \
  "ds_read_b64_tr_b16 v[90:91], v140 offset:18016\n" \
  "s_waitcnt lgkmcnt(4)\n" \
  "v_mfma_f32_16x16x32_bf16 v[176:179], v[76:79], v[124:127], v[176:179]\n" \
  "ds_read_b64_tr_b16 v[92:93], v140 offset:128\n" \
  "v_mfma_f32_16x16x32_bf16 v[180:183], v[76:79], v[128:131], v[180:183]\n" \
  "ds_read_b64_tr_b16 v[94:95], v140 offset:3712\n" \
  "v_mfma_f32_16x16x32_bf16 v[184:187], v[80:83], v[124:127], v[184:187]\n" \
  "ds_read_b64_tr_b16 v[96:97], v140 offset:14464\n" \
  "v_mfma_f32_16x16x32_bf16 v[188:191], v[80:83], v[128:131], v[188:191]\n" \
  "ds_read_b64_tr_b16 v[98:99], v140 offset:18048\n" \
  "s_waitcnt lgkmcnt(4)\n" \
  "v_mfma_f32_16x16x32_bf16 v[192:195], v[84:87], v[124:127], v[192:195]\n" \
  "ds_read_b64_tr_b16 v[100:101], v140 offset:160\n" \
  "v_mfma_f32_16x16x32_bf16 v[196:199], v[84:87], v[128:131], v[196:199]\n" \
  "ds_read_b64_tr_b16 v[102:103], v140 offset:3744\n" \
  "s_mov_b32 s53, 57344\n" \
  "buffer_load_dwordx4 v[116:119], %13, s[40:43], s53 offen offset:2048 sc1\n" \
  "v_mfma_f32_16x16x32_bf16 v[200:203], v[88:91], v[124:127], v[200:203]\n" \
  "ds_read_b64_tr_b16 v[104:105], v140 offset:14496\n" \
  "v_mfma_f32_16x16x32_bf16 v[204:207], v[88:91], v[128:131], v[204:207]\n" \
  "ds_read_b64_tr_b16 v[106:107], v140 offset:18080\n" \
  "s_mov_b32 s53, 57344\n" \
  "buffer_load_dwordx4 v[120:123], %13, s[40:43], s53 offen offset:3072 sc1\n" \
  "s_waitcnt lgkmcnt(4)\n" \
  "v_mfma_f32_16x16x32_bf16 v[208:211], v[92:95], v[124:127], v[208:211]\n" \
  "ds_read_b64_tr_b16 v[76:77], v140 offset:192\n" \
  "v_mfma_f32_16x16x32_bf16 v[212:215], v[92:95], v[128:131], v[212:215]\n" \
  "ds_read_b64_tr_b16 v[78:79], v140 offset:3776\n" \
  "v_mfma_f32_16x16x32_bf16 v[216:219], v[96:99], v[124:127], v[216:219]\n" \
  "ds_read_b64_tr_b16 v[80:81], v140 offset:14528\n" \
  "v_mfma_f32_16x16x32_bf16 v[220:223], v[96:99], v[128:131], v[220:223]\n" \
  "ds_read_b64_tr_b16 v[82:83], v140 offset:18112\n" \
  "s_waitcnt lgkmcnt(4)\n" \
  "v_mfma_f32_16x16x32_bf16 v[224:227], v[100:103], v[124:127], v[224:227]\n" \
  "s_setprio 0\n" \
  "ds_read_b64_tr_b16 v[84:85], v140 offset:7168\n" \
  "v_mfma_f32_16x16x32_bf16 v[228:231], v[100:103], v[128:131], v[228:231]\n" \
  "ds_read_b64_tr_b16 v[86:87], v140 offset:10752\n" \
  "v_mfma_f32_16x16x32_bf16 v[232:235], v[104:107], v[124:127], v[232:235]\n" \
  "ds_read_b64_tr_b16 v[88:89], v140 offset:21504\n" \
  "v_mfma_f32_16x16x32_bf16 v[236:239], v[104:107], v[128:131], v[236:239]\n" \
  "ds_read_b64_tr_b16 v[90:91], v140 offset:25088\n" \
  "s_waitcnt lgkmcnt(4)\n" \
  "v_mfma_f32_16x16x32_bf16 v[240:243], v[76:79], v[124:127], v[240:243]\n" \
  "ds_read_b64_tr_b16 v[92:93], v140 offset:7200\n" \
  "v_mfma_f32_16x16x32_bf16 v[244:247], v[76:79], v[128:131], v[244:247]\n" \
  "ds_read_b64_tr_b16 v[94:95], v140 offset:10784\n" \
  "v_mfma_f32_16x16x32_bf16 v[248:251], v[80:83], v[124:127], v[248:251]\n" \
  "ds_read_b64_tr_b16 v[96:97], v140 offset:21536\n" \
  "v_mfma_f32_16x16x32_bf16 v[252:255], v[80:83], v[128:131], v[252:255]\n" \
  "ds_read_b64_tr_b16 v[98:99], v140 offset:25120\n" \
  "s_waitcnt vmcnt(7) lgkmcnt(4)\n" \
  "v_mfma_f32_16x16x32_bf16 v[144:147], v[84:87], v[132:135], v[144:147]\n" \
  "ds_read_b64_tr_b16 v[100:101], v140 offset:7232\n" \
  "v_mfma_f32_16x16x32_bf16 v[148:151], v[84:87], v[136:139], v[148:151]\n" \
  "ds_read_b64_tr_b16 v[102:103], v140 offset:10816\n" \
  "v_mfma_f32_16x16x32_bf16 v[152:155], v[88:91], v[132:135], v[152:155]\n" \
  "ds_read_b64_tr_b16 v[104:105], v140 offset:21568\n" \
  "v_mfma_f32_16x16x32_bf16 v[156:159], v[88:91], v[136:139], v[156:159]\n" \
  "ds_read_b64_tr_b16 v[106:107], v140 offset:25152\n" \
  "s_waitcnt lgkmcnt(4)\n" \
  "v_mfma_f32_16x16x32_bf16 v[160:163], v[92:95], v[132:135], v[160:163]\n" \
  "ds_read_b64_tr_b16 v[76:77], v140 offset:7264\n" \
  "v_mfma_f32_16x16x32_bf16 v[164:167], v[92:95], v[136:139], v[164:167]\n" \
  "ds_read_b64_tr_b16 v[78:79], v140 offset:10848\n" \
  "v_mfma_f32_16x16x32_bf16 v[168:171], v[96:99], v[132:135], v[168:171]\n" \
  "ds_read_b64_tr_b16 v[80:81], v140 offset:21600\n" \
  "v_mfma_f32_16x16x32_bf16 v[172:175], v[96:99], v[136:139], v[172:175]\n" \
  "ds_read_b64_tr_b16 v[82:83], v140 offset:25184\n" \
  "s_waitcnt lgkmcnt(4)\n" \
  "v_mfma_f32_16x16x32_bf16 v[176:179], v[100:103], v[132:135], v[176:179]\n" \
  "ds_read_b64_tr_b16 v[84:85], v140 offset:7296\n" \
  "v_mfma_f32_16x16x32_bf16 v[180:183], v[100:103], v[136:139], v[180:183]\n" \
  "ds_read_b64_tr_b16 v[86:87], v140 offset:10880\n" \
  "v_mfma_f32_16x16x32_bf16 v[184:187], v[104:107], v[132:135], v[184:187]\n" \
  "ds_read_b64_tr_b16 v[88:89], v140 offset:21632\n" \
  "v_mfma_f32_16x16x32_bf16 v[188:191], v[104:107], v[136:139], v[188:191]\n" \
  "ds_read_b64_tr_b16 v[90:91], v140 offset:25216\n" \
  "s_waitcnt lgkmcnt(4)\n" \
  "v_mfma_f32_16x16x32_bf16 v[192:195], v[76:79], v[132:135], v[192:195]\n" \
  "ds_read_b64_tr_b16 v[92:93], v140 offset:7328\n" \
  "v_mfma_f32_16x16x32_bf16 v[196:199], v[76:79], v[136:139], v[196:199]\n" \
  "ds_read_b64_tr_b16 v[94:95], v140 offset:10912\n" \
  "s_mov_b32 s53, 61440\n" \
  "buffer_load_dwordx4 v[124:127], %13, s[40:43], s53 offen offset:0 sc1\n" \
  "v_mfma_f32_16x16x32_bf16 v[200:203], v[80:83], v[132:135], v[200:203]\n" \
  "ds_read_b64_tr_b16 v[96:97], v140 offset:21664\n" \
  "v_mfma_f32_16x16x32_bf16 v[204:207], v[80:83], v[136:139], v[204:207]\n" \
  "ds_read_b64_tr_b16 v[98:99], v140 offset:25248\n" \
  "s_mov_b32 s53, 61440\n" \
  "buffer_load_dwordx4 v[128:131], %13, s[40:43], s53 offen offset:1024 sc1\n" \
  "s_waitcnt lgkmcnt(4)\n" \
  "v_mfma_f32_16x16x32_bf16 v[208:211], v[84:87], v[132:135], v[208:211]\n" \
  "ds_read_b64_tr_b16 v[100:101], v140 offset:7360\n" \
  "v_mfma_f32_16x16x32_bf16 v[212:215], v[84:87], v[136:139], v[212:215]\n" \
  "ds_read_b64_tr_b16 v[102:103], v140 offset:10944\n" \
  "v_mfma_f32_16x16x32_bf16 v[216:219], v[88:91], v[132:135], v[216:219]\n" \
  "ds_read_b64_tr_b16 v[104:105], v140 offset:21696\n" \
  "v_mfma_f32_16x16x32_bf16 v[220:223], v[88:91], v[136:139], v[220:223]\n" \
  "ds_read_b64_tr_b16 v[106:107], v140 offset:25280\n" \
  "s_waitcnt lgkmcnt(4)\n" \
  "v_mfma_f32_16x16x32_bf16 v[224:227], v[92:95], v[132:135], v[224:227]\n" \
  "v_add_u32 v140, 114688, %14\n" \
  "s_waitcnt vmcnt(4)\n" \
  "s_barrier\n" \
  "ds_read_b64_tr_b16 v[76:77], v140 offset:0\n" \
  "v_mfma_f32_16x16x32_bf16 v[228:231], v[92:95], v[136:139], v[228:231]\n" \
  "ds_read_b64_tr_b16 v[78:79], v140 offset:3584\n" \
  "v_mfma_f32_16x16x32_bf16 v[232:235], v[96:99], v[132:135], v[232:235]\n" \
  "ds_read_b64_tr_b16 v[80:81], v140 offset:14336\n" \
  "v_mfma_f32_16x16x32_bf16 v[236:239], v[96:99], v[136:139], v[236:239]\n" \
  "ds_read_b64_tr_b16 v[82:83], v140 offset:17920\n" \
  "s_waitcnt lgkmcnt(4)\n" \
  "v_mfma_f32_16x16x32_bf16 v[240:243], v[100:103], v[132:135], v[240:243]\n" \
  "ds_read_b64_tr_b16 v[84:85], v140 offset:32\n" \
  "v_mfma_f32_16x16x32_bf16 v[244:247], v[100:103], v[136:139], v[244:247]\n" \
  "ds_read_b64_tr_b16 v[86:87], v140 offset:3616\n" \
  "v_mfma_f32_16x16x32_bf16 v[248:251], v[104:107], v[132:135], v[248:251]\n" \
  "ds_read_b64_tr_b16 v[88:89], v140 offset:14368\n" \
  "v_mfma_f32_16x16x32_bf16 v[252:255], v[104:107], v[136:139], v[252:255]\n" \
  "ds_read_b64_tr_b16 v[90:91], v140 offset:17952\n" \
  "s_mov_b32 s53, 61440\n" \
  "buffer_load_dwordx4 v[132:135], %13, s[40:43], s53 offen offset:2048 sc1\n" \
  "s_waitcnt vmcnt(6) lgkmcnt(4)\n" \
  "v_mfma_f32_16x16x32_bf16 v[144:147], v[76:79], v[108:111], v[144:147]\n" \
  "ds_read_b64_tr_b16 v[92:93], v140 offset:64\n" \
  "v_mfma_f32_16x16x32_bf16 v[148:151], v[76:79], v[112:115], v[148:151]\n" \
  "ds_read_b64_tr_b16 v[94:95], v140 offset:3648\n" \
  "s_mov_b32 s53, 61440\n" \
  "buffer_load_dwordx4 v[136:139], %13, s[40:43], s53 offen offset:3072 sc1\n" \
  "v_mfma_f32_16x16x32_bf16 v[152:155], v[80:83], v[108:111], v[152:155]\n" \
  "ds_read_b64_tr_b16 v[96:97], v140 offset:14400\n" \
  "v_mfma_f32_16x16x32_bf16 v[156:159], v[80:83], v[112:115], v[156:159]\n" \
  "ds_read_b64_tr_b16 v[98:99], v140 offset:17984\n" \
  "s_barrier\n" \
  "s_add_u32 m0, %23, 33792\n" \
  "s_mov_b32 s53, 0\n" \
  "buffer_load_dwordx4 %21, %22, s53 offen lds\n" \
  "s_waitcnt lgkmcnt(4)\n" \
  "v_mfma_f32_16x16x32_bf16 v[160:163], v[84:87], v[108:111], v[160:163]\n" \
  "ds_read_b64_tr_b16 v[100:101], v140 offset:96\n" \
  "v_mfma_f32_16x16x32_bf16 v[164:167], v[84:87], v[112:115], v[164:167]\n" \
  "ds_read_b64_tr_b16 v[102:103], v140 offset:3680\n" \
  "s_add_u32 m0, %23, 42240\n" \
  "s_mov_b32 s53, 65536\n" \
  "buffer_load_dwordx4 %21, %22, s53 offen lds\n" \
  "v_mfma_f32_16x16x32_bf16 v[168:171], v[88:91], v[108:111], v[168:171]\n" \
  "ds_read_b64_tr_b16 v[104:105], v140 offset:14432\n" \
  "v_mfma_f32_16x16x32_bf16 v[172:175], v[88:91], v[112:115], v[172:175]\n" \
  "ds_read_b64_tr_b16 v[106:107], v140 offset:18016\n" \
  "s_add_u32 m0, %23, 50688\n" \
  "s_mov_b32 s53, 131072\n" \
  "buffer_load_dwordx4 %21, %22, s53 offen lds\n" \
  "s_waitcnt lgkmcnt(4)\n" \
  "v_mfma_f32_16x16x32_bf16 v[176:179], v[92:95], v[108:111], v[176:179]\n" \
  "ds_read_b64_tr_b16 v[76:77], v140 offset:128\n" \
  "v_mfma_f32_16x16x32_bf16 v[180:183], v[92:95], v[112:115], v[180:183]\n" \
  "ds_read_b64_tr_b16 v[78:79], v140 offset:3712\n" \
  "s_add_u32 m0, %23, 59136\n" \
  "s_mov_b32 s53, 196608\n" \
  "buffer_load_dwordx4 %21, %22, s53 offen lds\n" \
  "v_mfma_f32_16x16x32_bf16 v[184:187], v[96:99], v[108:111], v[184:187]\n" \
  "ds_read_b64_tr_b16 v[80:81], v140 offset:14464\n" \
  "v_mfma_f32_16x16x32_bf16 v[188:191], v[96:99], v[112:115], v[188:191]\n" \
  "ds_read_b64_tr_b16 v[82:83], v140 offset:18048\n" \
  "s_add_u32 m0, %20, 0\n" \
  "s_nop 0\n" \
  "global_load_lds_dword %19, off\n" \
  "s_waitcnt lgkmcnt(4)\n" \
  "v_mfma_f32_16x16x32_bf16 v[192:195], v[100:103], v[108:111], v[192:195]\n" \
  "ds_read_b64_tr_b16 v[84:85], v140 offset:160\n" \
  "v_mfma_f32_16x16x32_bf16 v[196:199], v[100:103], v[112:115], v[196:199]\n" \
  "ds_read_b64_tr_b16 v[86:87], v140 offset:3744\n" \
  "v_mfma_f32_16x16x32_bf16 v[200:203], v[104:107], v[108:111], v[200:203]\n" \
  "ds_read_b64_tr_b16 v[88:89], v140 offset:14496\n" \
  "v_mfma_f32_16x16x32_bf16 v[204:207], v[104:107], v[112:115], v[204:207]\n" \
  "ds_read_b64_tr_b16 v[90:91], v140 offset:18080\n" \
  "s_waitcnt lgkmcnt(4)\n" \
  "v_mfma_f32_16x16x32_bf16 v[208:211], v[76:79], v[108:111], v[208:211]\n" \
  "ds_read_b64_tr_b16 v[92:93], v140 offset:192\n" \
  "v_mfma_f32_16x16x32_bf16 v[212:215], v[76:79], v[112:115], v[212:215]\n" \
  "ds_read_b64_tr_b16 v[94:95], v140 offset:3776\n" \
  "v_mfma_f32_16x16x32_bf16 v[216:219], v[80:83], v[108:111], v[216:219]\n" \
  "ds_read_b64_tr_b16 v[96:97], v140 offset:14528\n" \
  "v_mfma_f32_16x16x32_bf16 v[220:223], v[80:83], v[112:115], v[220:223]\n" \
  "ds_read_b64_tr_b16 v[98:99], v140 offset:18112\n" \
  "s_waitcnt lgkmcnt(4)\n" \
  "v_mfma_f32_16x16x32_bf16 v[224:227], v[84:87], v[108:111], v[224:227]\n" \
  "ds_read_b64_tr_b16 v[100:101], v140 offset:7168\n" \
  "v_mfma_f32_16x16x32_bf16 v[228:231], v[84:87], v[112:115], v[228:231]\n" \
  "ds_read_b64_tr_b16 v[102:103], v140 offset:10752\n" \
  "v_mfma_f32_16x16x32_bf16 v[232:235], v[88:91], v[108:111], v[232:235]\n" \
  "ds_read_b64_tr_b16 v[104:105], v140 offset:21504\n" \
  "v_mfma_f32_16x16x32_bf16 v[236:239], v[88:91], v[112:115], v[236:239]\n" \
  "ds_read_b64_tr_b16 v[106:107], v140 offset:25088\n" \
  "s_waitcnt lgkmcnt(4)\n" \
  "v_mfma_f32_16x16x32_bf16 v[240:243], v[92:95], v[108:111], v[240:243]\n" \
  "ds_read_b64_tr_b16 v[76:77], v140 offset:7200\n" \
  "v_mfma_f32_16x16x32_bf16 v[244:247], v[92:95], v[112:115], v[244:247]\n" \
  "ds_read_b64_tr_b16 v[78:79], v140 offset:10784\n" \
  "v_mfma_f32_16x16x32_bf16 v[248:251], v[96:99], v[108:111], v[248:251]\n" \
  "ds_read_b64_tr_b16 v[80:81], v140 offset:21536\n" \
  "v_mfma_f32_16x16x32_bf16 v[252:255], v[96:99], v[112:115], v[252:255]\n" \
  "ds_read_b64_tr_b16 v[82:83], v140 offset:25120\n" \
  "s_waitcnt vmcnt(9) lgkmcnt(4)\n" \
  "v_mfma_f32_16x16x32_bf16 v[144:147], v[100:103], v[116:119], v[144:147]\n" \
  "ds_read_b64_tr_b16 v[84:85], v140 offset:7232\n" \
  "v_mfma_f32_16x16x32_bf16 v[148:151], v[100:103], v[120:123], v[148:151]\n" \
  "ds_read_b64_tr_b16 v[86:87], v140 offset:10816\n" \
  "v_mfma_f32_16x16x32_bf16 v[152:155], v[104:107], v[116:119], v[152:155]\n" \
  "ds_read_b64_tr_b16 v[88:89], v140 offset:21568\n" \
  "v_mfma_f32_16x16x32_bf16 v[156:159], v[104:107], v[120:123], v[156:159]\n" \
  "ds_read_b64_tr_b16 v[90:91], v140 offset:25152\n" \
  "s_waitcnt lgkmcnt(4)\n" \
  "v_mfma_f32_16x16x32_bf16 v[160:163], v[76:79], v[116:119], v[160:163]\n" \
  "ds_read_b64_tr_b16 v[92:93], v140 offset:7264\n" \
  "v_mfma_f32_16x16x32_bf16 v[164:167], v[76:79], v[120:123], v[164:167]\n" \
  "ds_read_b64_tr_b16 v[94:95], v140 offset:10848\n" \
  "v_mfma_f32_16x16x32_bf16 v[168:171], v[80:83], v[116:119], v[168:171]\n" \
  "ds_read_b64_tr_b16 v[96:97], v140 offset:21600\n" \
  "v_mfma_f32_16x16x32_bf16 v[172:175], v[80:83], v[120:123], v[172:175]\n" \
  "ds_read_b64_tr_b16 v[98:99], v140 offset:25184\n" \
  "s_waitcnt lgkmcnt(4)\n" \
  "v_mfma_f32_16x16x32_bf16 v[176:179], v[84:87], v[116:119], v[176:179]\n" \
  "ds_read_b64_tr_b16 v[100:101], v140 offset:7296\n" \
  "v_mfma_f32_16x16x32_bf16 v[180:183], v[84:87], v[120:123], v[180:183]\n" \
  "ds_read_b64_tr_b16 v[102:103], v140 offset:10880\n" \
  "v_mfma_f32_16x16x32_bf16 v[184:187], v[88:91], v[116:119], v[184:187]\n" \
  "ds_read_b64_tr_b16 v[104:105], v140 offset:21632\n" \
  "v_mfma_f32_16x16x32_bf16 v[188:191], v[88:91], v[120:123], v[188:191]\n" \
  "ds_read_b64_tr_b16 v[106:107], v140 offset:25216\n" \
  "s_waitcnt lgkmcnt(4)\n" \
  "v_mfma_f32_16x16x32_bf16 v[192:195], v[92:95], v[116:119], v[192:195]\n" \
  "ds_read_b64_tr_b16 v[76:77], v140 offset:7328\n" \
  "v_mfma_f32_16x16x32_bf16 v[196:199], v[92:95], v[120:123], v[196:199]\n" \
  "ds_read_b64_tr_b16 v[78:79], v140 offset:10912\n" \
  "v_mfma_f32_16x16x32_bf16 v[200:203], v[96:99], v[116:119], v[200:203]\n" \
  "ds_read_b64_tr_b16 v[80:81], v140 offset:21664\n" \
  "v_mfma_f32_16x16x32_bf16 v[204:207], v[96:99], v[120:123], v[204:207]\n" \
  "ds_read_b64_tr_b16 v[82:83], v140 offset:25248\n" \
  "s_waitcnt lgkmcnt(4)\n" \
  "v_mfma_f32_16x16x32_bf16 v[208:211], v[100:103], v[116:119], v[208:211]\n" \
  "ds_read_b64_tr_b16 v[84:85], v140 offset:7360\n" \
  "v_mfma_f32_16x16x32_bf16 v[212:215], v[100:103], v[120:123], v[212:215]\n" \
  "ds_read_b64_tr_b16 v[86:87], v140 offset:10944\n" \
  "v_mfma_f32_16x16x32_bf16 v[216:219], v[104:107], v[116:119], v[216:219]\n" \
  "ds_read_b64_tr_b16 v[88:89], v140 offset:21696\n" \
  "v_mfma_f32_16x16x32_bf16 v[220:223], v[104:107], v[120:123], v[220:223]\n" \
  "ds_read_b64_tr_b16 v[90:91], v140 offset:25280\n" \
  "s_waitcnt lgkmcnt(4)\n" \
  "v_mfma_f32_16x16x32_bf16 v[224:227], v[76:79], v[116:119], v[224:227]\n" \
  "v_add_u32 v140, 0, %14\n" \
  "ds_read_b64_tr_b16 v[92:93], v140 offset:0\n" \
  "v_mfma_f32_16x16x32_bf16 v[228:231], v[76:79], v[120:123], v[228:231]\n" \
  "ds_read_b64_tr_b16 v[94:95], v140 offset:3584\n" \
  "v_mfma_f32_16x16x32_bf16 v[232:235], v[80:83], v[116:119], v[232:235]\n" \
  "ds_read_b64_tr_b16 v[96:97], v140 offset:14336\n" \
  "v_mfma_f32_16x16x32_bf16 v[236:239], v[80:83], v[120:123], v[236:239]\n" \
  "ds_read_b64_tr_b16 v[98:99], v140 offset:17920\n" \
  "s_waitcnt lgkmcnt(4)\n" \
  "v_mfma_f32_16x16x32_bf16 v[240:243], v[84:87], v[116:119], v[240:243]\n" \
  "ds_read_b64_tr_b16 v[100:101], v140 offset:32\n" \
  "v_mfma_f32_16x16x32_bf16 v[244:247], v[84:87], v[120:123], v[244:247]\n" \
  "ds_read_b64_tr_b16 v[102:103], v140 offset:3616\n" \
  "v_mfma_f32_16x16x32_bf16 v[248:251], v[88:91], v[116:119], v[248:251]\n" \
  "ds_read_b64_tr_b16 v[104:105], v140 offset:14368\n" \
  "v_mfma_f32_16x16x32_bf16 v[252:255], v[88:91], v[120:123], v[252:255]\n" \
  "ds_read_b64_tr_b16 v[106:107], v140 offset:17952\n" \
  "s_waitcnt vmcnt(7) lgkmcnt(4)\n" \
  "v_mfma_f32_16x16x32_bf16 v[144:147], v[92:95], v[124:127], v[144:147]\n" \
  "ds_read_b64_tr_b16 v[76:77], v140 offset:64\n" \
  "v_mfma_f32_16x16x32_bf16 v[148:151], v[92:95], v[128:131], v[148:151]\n" \
  "ds_read_b64_tr_b16 v[78:79], v140 offset:3648\n" \
  "v_mfma_f32_16x16x32_bf16 v[152:155], v[96:99], v[124:127], v[152:155]\n" \
  "ds_read_b64_tr_b16 v[80:81], v140 offset:14400\n" \
  "v_mfma_f32_16x16x32_bf16 v[156:159], v[96:99], v[128:131], v[156:159]\n" \
  "ds_read_b64_tr_b16 v[82:83], v140 offset:17984\n" \
  "s_waitcnt lgkmcnt(4)\n" \
  "v_mfma_f32_16x16x32_bf16 v[160:163], v[100:103], v[124:127], v[160:163]\n" \
  "ds_read_b64_tr_b16 v[84:85], v140 offset:96\n" \
  "v_mfma_f32_16x16x32_bf16 v[164:167], v[100:103], v[128:131], v[164:167]\n" \
  "ds_read_b64_tr_b16 v[86:87], v140 offset:3680\n" \
  "v_mfma_f32_16x16x32_bf16 v[168:171], v[104:107], v[124:127], v[168:171]\n" \
  "ds_read_b64_tr_b16 v[88:89], v140 offset:14432\n" \
  "v_mfma_f32_16x16x32_bf16 v[172:175], v[104:107], v[128:131], v[172:175]\n" \
  "ds_read_b64_tr_b16 v[90:91], v140 offset:18016\n" \
  "s_waitcnt lgkmcnt(4)\n" \
  "v_mfma_f32_16x16x32_bf16 v[176:179], v[76:79], v[124:127], v[176:179]\n" \
  "ds_read_b64_tr_b16 v[92:93], v140 offset:128\n" \
  "v_mfma_f32_16x16x32_bf16 v[180:183], v[76:79], v[128:131], v[180:183]\n" \
  "ds_read_b64_tr_b16 v[94:95], v140 offset:3712\n" \
  "v_mfma_f32_16x16x32_bf16 v[184:187], v[80:83], v[124:127], v[184:187]\n" \
  "ds_read_b64_tr_b16 v[96:97], v140 offset:14464\n" \
  "v_mfma_f32_16x16x32_bf16 v[188:191], v[80:83], v[128:131], v[188:191]\n" \
  "ds_read_b64_tr_b16 v[98:99], v140 offset:18048\n" \
  "s_waitcnt lgkmcnt(4)\n" \
  "v_mfma_f32_16x16x32_bf16 v[192:195], v[84:87], v[124:127], v[192:195]\n" \
  "ds_read_b64_tr_b16 v[100:101], v140 offset:160\n" \
  "v_mfma_f32_16x16x32_bf16 v[196:199], v[84:87], v[128:131], v[196:199]\n" \
  "ds_read_b64_tr_b16 v[102:103], v140 offset:3744\n" \
  "v_mfma_f32_16x16x32_bf16 v[200:203], v[88:91], v[124:127], v[200:203]\n" \
  "ds_read_b64_tr_b16 v[104:105], v140 offset:14496\n" \
  "v_mfma_f32_16x16x32_bf16 v[204:207], v[88:91], v[128:131], v[204:207]\n" \
  "ds_read_b64_tr_b16 v[106:107], v140 offset:18080\n" \
  "s_waitcnt lgkmcnt(4)\n" \
  "v_mfma_f32_16x16x32_bf16 v[208:211], v[92:95], v[124:127], v[208:211]\n" \
  "ds_read_b64_tr_b16 v[76:77], v140 offset:192\n" \
  "v_mfma_f32_16x16x32_bf16 v[212:215], v[92:95], v[128:131], v[212:215]\n" \
  "ds_read_b64_tr_b16 v[78:79], v140 offset:3776\n" \
  "v_mfma_f32_16x16x32_bf16 v[216:219], v[96:99], v[124:127], v[216:219]\n" \
  "ds_read_b64_tr_b16 v[80:81], v140 offset:14528\n" \
  "v_mfma_f32_16x16x32_bf16 v[220:223], v[96:99], v[128:131], v[220:223]\n" \
  "ds_read_b64_tr_b16 v[82:83], v140 offset:18112\n" \
  "s_waitcnt lgkmcnt(4)\n" \
  "v_mfma_f32_16x16x32_bf16 v[224:227], v[100:103], v[124:127], v[224:227]\n" \
  "ds_read_b64_tr_b16 v[84:85], v140 offset:7168\n" \
  "v_mfma_f32_16x16x32_bf16 v[228:231], v[100:103], v[128:131], v[228:231]\n" \
  "ds_read_b64_tr_b16 v[86:87], v140 offset:10752\n" \
  "v_mfma_f32_16x16x32_bf16 v[232:235], v[104:107], v[124:127], v[232:235]\n" \
  "ds_read_b64_tr_b16 v[88:89], v140 offset:21504\n" \
  "v_mfma_f32_16x16x32_bf16 v[236:239], v[104:107], v[128:131], v[236:239]\n" \
  "ds_read_b64_tr_b16 v[90:91], v140 offset:25088\n" \
  "s_waitcnt lgkmcnt(4)\n" \
  "v_mfma_f32_16x16x32_bf16 v[240:243], v[76:79], v[124:127], v[240:243]\n" \
  "ds_read_b64_tr_b16 v[92:93], v140 offset:7200\n" \
  "v_mfma_f32_16x16x32_bf16 v[244:247], v[76:79], v[128:131], v[244:247]\n" \
  "ds_read_b64_tr_b16 v[94:95], v140 offset:10784\n" \
  "v_mfma_f32_16x16x32_bf16 v[248:251], v[80:83], v[124:127], v[248:251]\n" \
  "ds_read_b64_tr_b16 v[96:97], v140 offset:21536\n" \
  "v_mfma_f32_16x16x32_bf16 v[252:255], v[80:83], v[128:131], v[252:255]\n" \
  "ds_read_b64_tr_b16 v[98:99], v140 offset:25120\n" \
  "s_waitcnt vmcnt(5) lgkmcnt(4)\n" \
  "v_mfma_f32_16x16x32_bf16 v[144:147], v[84:87], v[132:135], v[144:147]\n" \
  "ds_read_b64_tr_b16 v[100:101], v140 offset:7232\n" \
  "v_mfma_f32_16x16x32_bf16 v[148:151], v[84:87], v[136:139], v[148:151]\n" \
  "ds_read_b64_tr_b16 v[102:103], v140 offset:10816\n" \
  "v_mfma_f32_16x16x32_bf16 v[152:155], v[88:91], v[132:135], v[152:155]\n" \
  "ds_read_b64_tr_b16 v[104:105], v140 offset:21568\n" \
  "v_mfma_f32_16x16x32_bf16 v[156:159], v[88:91], v[136:139], v[156:159]\n" \
  "ds_read_b64_tr_b16 v[106:107], v140 offset:25152\n" \
  "s_waitcnt lgkmcnt(4)\n" \
  "v_mfma_f32_16x16x32_bf16 v[160:163], v[92:95], v[132:135], v[160:163]\n" \
  "ds_read_b64_tr_b16 v[76:77], v140 offset:7264\n" \
  "v_mfma_f32_16x16x32_bf16 v[164:167], v[92:95], v[136:139], v[164:167]\n" \
  "ds_read_b64_tr_b16 v[78:79], v140 offset:10848\n" \
  "v_mfma_f32_16x16x32_bf16 v[168:171], v[96:99], v[132:135], v[168:171]\n" \
  "ds_read_b64_tr_b16 v[80:81], v140 offset:21600\n" \
  "v_mfma_f32_16x16x32_bf16 v[172:175], v[96:99], v[136:139], v[172:175]\n" \
  "ds_read_b64_tr_b16 v[82:83], v140 offset:25184\n" \
  "s_waitcnt lgkmcnt(4)\n" \
  "v_mfma_f32_16x16x32_bf16 v[176:179], v[100:103], v[132:135], v[176:179]\n" \
  "ds_read_b64_tr_b16 v[84:85], v140 offset:7296\n" \
  "v_mfma_f32_16x16x32_bf16 v[180:183], v[100:103], v[136:139], v[180:183]\n" \
  "ds_read_b64_tr_b16 v[86:87], v140 offset:10880\n" \
  "v_mfma_f32_16x16x32_bf16 v[184:187], v[104:107], v[132:135], v[184:187]\n" \
  "ds_read_b64_tr_b16 v[88:89], v140 offset:21632\n" \
  "v_mfma_f32_16x16x32_bf16 v[188:191], v[104:107], v[136:139], v[188:191]\n" \
  "ds_read_b64_tr_b16 v[90:91], v140 offset:25216\n" \
  "s_waitcnt lgkmcnt(4)\n" \
  "v_mfma_f32_16x16x32_bf16 v[192:195], v[76:79], v[132:135], v[192:195]\n" \
  "ds_read_b64_tr_b16 v[92:93], v140 offset:7328\n" \
  "v_mfma_f32_16x16x32_bf16 v[196:199], v[76:79], v[136:139], v[196:199]\n" \
  "ds_read_b64_tr_b16 v[94:95], v140 offset:10912\n" \
  "v_mfma_f32_16x16x32_bf16 v[200:203], v[80:83], v[132:135], v[200:203]\n" \
  "ds_read_b64_tr_b16 v[96:97], v140 offset:21664\n" \
  "v_mfma_f32_16x16x32_bf16 v[204:207], v[80:83], v[136:139], v[204:207]\n" \
  "ds_read_b64_tr_b16 v[98:99], v140 offset:25248\n" \
  "s_waitcnt lgkmcnt(4)\n" \
  "v_mfma_f32_16x16x32_bf16 v[208:211], v[84:87], v[132:135], v[208:211]\n" \
  "ds_read_b64_tr_b16 v[100:101], v140 offset:7360\n" \
  "v_mfma_f32_16x16x32_bf16 v[212:215], v[84:87], v[136:139], v[212:215]\n" \
  "ds_read_b64_tr_b16 v[102:103], v140 offset:10944\n" \
  "v_mfma_f32_16x16x32_bf16 v[216:219], v[88:91], v[132:135], v[216:219]\n" \
  "ds_read_b64_tr_b16 v[104:105], v140 offset:21696\n" \
  "v_mfma_f32_16x16x32_bf16 v[220:223], v[88:91], v[136:139], v[220:223]\n" \
  "ds_read_b64_tr_b16 v[106:107], v140 offset:25280\n" \
  "s_waitcnt lgkmcnt(4)\n" \
  "v_mfma_f32_16x16x32_bf16 v[224:227], v[92:95], v[132:135], v[224:227]\n" \
  "v_mfma_f32_16x16x32_bf16 v[228:231], v[92:95], v[136:139], v[228:231]\n" \
  "v_mfma_f32_16x16x32_bf16 v[232:235], v[96:99], v[132:135], v[232:235]\n" \
  "v_mfma_f32_16x16x32_bf16 v[236:239], v[96:99], v[136:139], v[236:239]\n" \
  "s_waitcnt lgkmcnt(0)\n" \
  "v_mfma_f32_16x16x32_bf16 v[240:243], v[100:103], v[132:135], v[240:243]\n" \
  "v_mfma_f32_16x16x32_bf16 v[244:247], v[100:103], v[136:139], v[244:247]\n" \
  "v_mfma_f32_16x16x32_bf16 v[248:251], v[104:107], v[132:135], v[248:251]\n" \
  "v_mfma_f32_16x16x32_bf16 v[252:255], v[104:107], v[136:139], v[252:255]\n" \
  "s_nop 15\n"
#define K4_CLOB "v76", "v77", "v78", "v79", "v80", "v81", "v82", "v83", "v84", "v85", "v86", "v87", "v88", "v89", "v90", "v91", "v92", "v93", "v94", "v95", "v96", "v97", "v98", "v99", "v100", "v101", "v102", "v103", "v104", "v105", "v106", "v107", "v108", "v109", "v110", "v111", "v112", "v113", "v114", "v115", "v116", "v117", "v118", "v119", "v120", "v121", "v122", "v123", "v124", "v125", "v126", "v127", "v128", "v129", "v130", "v131", "v132", "v133", "v134", "v135", "v136", "v137", "v138", "v139", "v140", "s40", "s41", "s42", "s43", "s44", "s45", "s46", "s47", "s48", "s49", "s50", "s51", "s52", "s53", "scc", "memory"
#define K4_OUT(a, x) "=&{v[144:159]}"((a)[0]), "=&{v[160:175]}"((a)[1]), "=&{v[176:191]}"((a)[2]), "=&{v[192:207]}"((a)[3]), "=&{v[208:223]}"((a)[4]), "=&{v[224:239]}"((a)[5]), "=&{v[240:255]}"((a)[6])
#define K4_LDS 143360
#define K4_LAST 0
#define K4_SS 28672
#define K4_NBAR 15
#define K5_PRE0 \
  "s_nop 4\n" \
  "s_mov_b64 s[44:45], %0\n" \
  "s_mov_b32 s46, %1\n" \
  "s_mov_b32 s47, 0x20000\n" \
  "s_and_b32 s45, s45, 0xffff\n" \
  "s_add_u32 m0, %2, 33792\n" \
  "s_mov_b32 s53, 0\n" \
  "buffer_load_dwordx4 %4, s[44:47], s53 offen lds\n" \
  "s_add_u32 m0, %2, 42240\n" \
  "s_mov_b32 s53, 0\n" \
  "buffer_load_dwordx4 %5, s[44:47], s53 offen lds\n" \
  "s_add_u32 m0, %2, 50688\n" \
  "s_mov_b32 s53, 0\n" \
  "buffer_load_dwordx4 %6, s[44:47], s53 offen lds\n" \
  "s_add_u32 m0, %2, 59136\n" \
  "s_mov_b32 s53, 0\n" \
  "buffer_load_dwordx4 %7, s[44:47], s53 offen lds\n"
#define K5_PRE1 \
  "s_nop 4\n" \
  "s_mov_b64 s[44:45], %0\n" \
  "s_mov_b32 s46, %1\n" \
  "s_mov_b32 s47, 0x20000\n" \
  "s_and_b32 s45, s45, 0xffff\n" \
  "s_add_u32 m0, %2, 67584\n" \
  "s_mov_b32 s53, 262144\n" \
  "buffer_load_dwordx4 %4, s[44:47], s53 offen lds\n" \
  "s_add_u32 m0, %2, 76032\n" \
  "s_mov_b32 s53, 262144\n" \
  "buffer_load_dwordx4 %5, s[44:47], s53 offen lds\n" \
  "s_add_u32 m0, %2, 84480\n" \
  "s_mov_b32 s53, 262144\n" \
  "buffer_load_dwordx4 %6, s[44:47], s53 offen lds\n" \
  "s_add_u32 m0, %2, 92928\n" \
  "s_mov_b32 s53, 262144\n" \
  "buffer_load_dwordx4 %7, s[44:47], s53 offen lds\n" \
  "s_add_u32 m0, %2, 101376\n" \
  "s_mov_b32 s53, 524288\n" \
  "buffer_load_dwordx4 %4, s[44:47], s53 offen lds\n" \
  "s_add_u32 m0, %2, 109824\n" \
  "s_mov_b32 s53, 524288\n" \
  "buffer_load_dwordx4 %5, s[44:47], s53 offen lds\n" \
  "s_add_u32 m0, %2, 118272\n" \
  "s_mov_b32 s53, 524288\n" \
  "buffer_load_dwordx4 %6, s[44:47], s53 offen lds\n" \
  "s_add_u32 m0, %2, 126720\n" \
  "s_mov_b32 s53, 524288\n" \
  "buffer_load_dwordx4 %7, s[44:47], s53 offen lds\n" \
  "s_add_u32 m0, %2, 0\n" \
  "s_mov_b32 s53, 786432\n" \
  "buffer_load_dwordx4 %4, s[44:47], s53 offen lds\n" \
  "s_add_u32 m0, %2, 8448\n" \
  "s_mov_b32 s53, 786432\n" \
  "buffer_load_dwordx4 %5, s[44:47], s53 offen lds\n" \
  "s_add_u32 m0, %2, 16896\n" \
  "s_mov_b32 s53, 786432\n" \
  "buffer_load_dwordx4 %6, s[44:47], s53 offen lds\n" \
  "s_add_u32 m0, %2, 25344\n" \
  "s_mov_b32 s53, 786432\n" \
  "buffer_load_dwordx4 %7, s[44:47], s53 offen lds\n"
#define K5_NPRE 16
#define K5_PRECLOB "s40", "s41", "s42", "s43", "s44", "s45", "s46", "s47", "s48", "s49", "s50", "s51", "s52", "s53", "scc", "memory"
#define K5_DEF \
  "s_waitcnt lgkmcnt(0)\n" \
  "v_add_u32 v160, 33792, %4\n" \
  "ds_read_b64_tr_b16 v[96:97], v160 offset:16896\n" \
  "ds_read_b64_tr_b16 v[98:99], v160 offset:17152\n" \
  "ds_read_b64_tr_b16 v[100:101], v160 offset:16928\n" \
  "ds_read_b64_tr_b16 v[102:103], v160 offset:17184\n" \
  "ds_read_b64_tr_b16 v[104:105], v160 offset:16960\n" \
  "ds_read_b64_tr_b16 v[106:107], v160 offset:17216\n" \
  "ds_read_b64_tr_b16 v[108:109], v160 offset:16992\n" \
  "ds_read_b64_tr_b16 v[110:111], v160 offset:17248\n" \
  "s_waitcnt lgkmcnt(6)\n" \
  "v_mfma_f32_16x16x32_bf16 v[176:179], v[96:99], v[136:139], v[176:179]\n" \
  "v_add_u32 v160, 67584, %4\n" \
  "ds_read_b64_tr_b16 v[112:113], v160 offset:0\n" \
  "v_mfma_f32_16x16x32_bf16 v[208:211], v[96:99], v[140:143], v[208:211]\n" \
  "ds_read_b64_tr_b16 v[114:115], v160 offset:256\n" \
  "s_waitcnt lgkmcnt(6)\n" \
  "v_mfma_f32_16x16x32_bf16 v[180:183], v[100:103], v[136:139], v[180:183]\n" \
  "ds_read_b64_tr_b16 v[116:117], v160 offset:32\n" \
  "v_mfma_f32_16x16x32_bf16 v[212:215], v[100:103], v[140:143], v[212:215]\n" \
  "ds_read_b64_tr_b16 v[118:119], v160 offset:288\n" \
  "s_waitcnt lgkmcnt(6)\n" \
  "v_mfma_f32_16x16x32_bf16 v[184:187], v[104:107], v[136:139], v[184:187]\n" \
  "ds_read_b64_tr_b16 v[120:121], v160 offset:64\n" \
  "v_mfma_f32_16x16x32_bf16 v[216:219], v[104:107], v[140:143], v[216:219]\n" \
  "ds_read_b64_tr_b16 v[122:123], v160 offset:320\n" \
  "s_waitcnt lgkmcnt(6)\n" \
  "v_mfma_f32_16x16x32_bf16 v[188:191], v[108:111], v[136:139], v[188:191]\n" \
  "ds_read_b64_tr_b16 v[124:125], v160 offset:96\n" \
  "v_mfma_f32_16x16x32_bf16 v[220:223], v[108:111], v[140:143], v[220:223]\n" \
  "ds_read_b64_tr_b16 v[126:127], v160 offset:352\n" \
  "s_waitcnt lgkmcnt(6)\n" \
  "v_mfma_f32_16x16x32_bf16 v[176:179], v[112:115], v[144:147], v[176:179]\n" \
  "ds_read_b64_tr_b16 v[96:97], v160 offset:16896\n" \
  "v_mfma_f32_16x16x32_bf16 v[208:211], v[112:115], v[148:151], v[208:211]\n" \
  "ds_read_b64_tr_b16 v[98:99], v160 offset:17152\n" \
  "s_waitcnt lgkmcnt(6)\n" \
  "v_mfma_f32_16x16x32_bf16 v[180:183], v[116:119], v[144:147], v[180:183]\n" \
  "ds_read_b64_tr_b16 v[100:101], v160 offset:16928\n" \
  "v_mfma_f32_16x16x32_bf16 v[212:215], v[116:119], v[148:151], v[212:215]\n" \
  "ds_read_b64_tr_b16 v[102:103], v160 offset:17184\n" \
  "s_waitcnt lgkmcnt(6)\n" \
  "v_mfma_f32_16x16x32_bf16 v[184:187], v[120:123], v[144:147], v[184:187]\n" \
  "ds_read_b64_tr_b16 v[104:105], v160 offset:16960\n" \
  "v_mfma_f32_16x16x32_bf16 v[216:219], v[120:123], v[148:151], v[216:219]\n" \
  "ds_read_b64_tr_b16 v[106:107], v160 offset:17216\n" \
  "s_waitcnt lgkmcnt(6)\n" \
  "v_mfma_f32_16x16x32_bf16 v[188:191], v[124:127], v[144:147], v[188:191]\n" \
  "ds_read_b64_tr_b16 v[108:109], v160 offset:16992\n" \
  "v_mfma_f32_16x16x32_bf16 v[220:223], v[124:127], v[148:151], v[220:223]\n" \
  "ds_read_b64_tr_b16 v[110:111], v160 offset:17248\n" \
  "s_waitcnt lgkmcnt(6)\n" \
  "v_mfma_f32_16x16x32_bf16 v[176:179], v[96:99], v[152:155], v[176:179]\n" \
  "v_mfma_f32_16x16x32_bf16 v[208:211], v[96:99], v[156:159], v[208:211]\n" \
  "s_waitcnt lgkmcnt(4)\n" \
  "v_mfma_f32_16x16x32_bf16 v[180:183], v[100:103], v[152:155], v[180:183]\n" \
  "v_mfma_f32_16x16x32_bf16 v[212:215], v[100:103], v[156:159], v[212:215]\n" \
  "s_waitcnt lgkmcnt(2)\n" \
  "v_mfma_f32_16x16x32_bf16 v[184:187], v[104:107], v[152:155], v[184:187]\n" \
  "v_mfma_f32_16x16x32_bf16 v[216:219], v[104:107], v[156:159], v[216:219]\n" \
  "s_waitcnt lgkmcnt(0)\n" \
  "v_mfma_f32_16x16x32_bf16 v[188:191], v[108:111], v[152:155], v[188:191]\n" \
  "v_mfma_f32_16x16x32_bf16 v[220:223], v[108:111], v[156:159], v[220:223]\n" \
  "s_nop 15\n"
#define K5_DEFIO(a) "+{v[176:191]}"((a)[0]), "+{v[208:223]}"((a)[2])
#define K5_DEFIN(f) "{v[128:143]}"((f)[0]), "{v[144:159]}"((f)[1])
#define K5_DEFCLOB "v96", "v97", "v98", "v99", "v100", "v101", "v102", "v103", "v104", "v105", "v106", "v107", "v108", "v109", "v110", "v111", "v112", "v113", "v114", "v115", "v116", "v117", "v118", "v119", "v120", "v121", "v122", "v123", "v124", "v125", "v126", "v127", "v160", "memory"
#define K5_ASM \
  "s_nop 4\n" \
  "s_mov_b64 s[44:45], %8\n" \
  "s_mov_b32 s46, %9\n" \
  "s_mov_b32 s47, 0x20000\n" \
  "s_mov_b64 s[40:41], %6\n" \
  "s_mov_b32 s42, %7\n" \
  "s_mov_b32 s43, 0x20000\n" \
  "s_and_b32 s41, s41, 0xffff\n" \
  "s_and_b32 s45, s45, 0xffff\n" \
  "s_mov_b32 s53, 0\n" \
  "buffer_load_dwordx4 v[128:131], %12, s[40:43], s53 offen offset:0 sc1\n" \
  "s_mov_b32 s53, 0\n" \
  "buffer_load_dwordx4 v[132:135], %18, s[40:43], s53 offen offset:0 sc1\n" \
  "s_mov_b32 s53, 4096\n" \
  "buffer_load_dwordx4 v[136:139], %12, s[40:43], s53 offen offset:0 sc1\n" \
  "s_mov_b32 s53, 4096\n" \
  "buffer_load_dwordx4 v[140:143], %18, s[40:43], s53 offen offset:0 sc1\n" \
  "s_mov_b32 s53, 8192\n" \
  "buffer_load_dwordx4 v[144:147], %12, s[40:43], s53 offen offset:0 sc1\n" \
  "s_mov_b32 s53, 8192\n" \
  "buffer_load_dwordx4 v[148:151], %18, s[40:43], s53 offen offset:0 sc1\n" \
  "s_mov_b32 s48, 40960\n" \
  "s_mov_b32 s49, 1572864\n" \
  "s_mov_b32 s50, 33792\n" \
  "s_add_u32 s51, %10, 101376\n" \
  "s_mov_b32 s52, 4\n" \
  "v_add_u32 v160, 33792, %13\n" \
  "s_waitcnt lgkmcnt(0)\n" \
  "ds_read_b64_tr_b16 v[96:97], v160 offset:0\n" \
  "ds_read_b64_tr_b16 v[98:99], v160 offset:256\n" \
  "ds_read_b64_tr_b16 v[100:101], v160 offset:32\n" \
  "ds_read_b64_tr_b16 v[102:103], v160 offset:288\n" \
  "ds_read_b64_tr_b16 v[104:105], v160 offset:64\n" \
  "ds_read_b64_tr_b16 v[106:107], v160 offset:320\n" \
  "ds_read_b64_tr_b16 v[108:109], v160 offset:96\n" \
  "ds_read_b64_tr_b16 v[110:111], v160 offset:352\n" \
  "s_waitcnt vmcnt(4) lgkmcnt(4)\n" \
  "v_mfma_f32_16x16x32_bf16 v[176:179], v[96:99], v[128:131], 0\n" \
  "s_setprio 2\n" \
  "ds_read_b64_tr_b16 v[112:113], v160 offset:128\n" \
  "v_mfma_f32_16x16x32_bf16 v[208:211], v[96:99], v[132:135], 0\n" \
  "ds_read_b64_tr_b16 v[114:115], v160 offset:384\n" \
  "v_mfma_f32_16x16x32_bf16 v[180:183], v[100:103], v[128:131], 0\n" \
  "ds_read_b64_tr_b16 v[116:117], v160 offset:160\n" \
  "v_mfma_f32_16x16x32_bf16 v[212:215], v[100:103], v[132:135], 0\n" \
  "ds_read_b64_tr_b16 v[118:119], v160 offset:416\n" \
  "s_waitcnt lgkmcnt(4)\n" \
  "v_mfma_f32_16x16x32_bf16 v[184:187], v[104:107], v[128:131], 0\n" \
  "ds_read_b64_tr_b16 v[120:121], v160 offset:192\n" \
  "v_mfma_f32_16x16x32_bf16 v[216:219], v[104:107], v[132:135], 0\n" \
  "ds_read_b64_tr_b16 v[122:123], v160 offset:448\n" \
  "s_mov_b32 s53, 12288\n" \
  "buffer_load_dwordx4 v[152:155], %12, s[40:43], s53 offen offset:0 sc1\n" \
  "v_mfma_f32_16x16x32_bf16 v[188:191], v[108:111], v[128:131], 0\n" \
  "ds_read_b64_tr_b16 v[124:125], v160 offset:224\n" \
  "v_mfma_f32_16x16x32_bf16 v[220:223], v[108:111], v[132:135], 0\n" \
  "ds_read_b64_tr_b16 v[126:127], v160 offset:480\n" \
  "s_mov_b32 s53, 12288\n" \
  "buffer_load_dwordx4 v[156:159], %18, s[40:43], s53 offen offset:0 sc1\n" \
  "s_waitcnt lgkmcnt(4)\n" \
  "v_mfma_f32_16x16x32_bf16 v[192:195], v[112:115], v[128:131], 0\n" \
  "s_setprio 1\n" \
  "ds_read_b64_tr_b16 v[96:97], v160 offset:16896\n" \
  "v_mfma_f32_16x16x32_bf16 v[224:227], v[112:115], v[132:135], 0\n" \
  "ds_read_b64_tr_b16 v[98:99], v160 offset:17152\n" \
  "v_mfma_f32_16x16x32_bf16 v[196:199], v[116:119], v[128:131], 0\n" \
  "ds_read_b64_tr_b16 v[100:101], v160 offset:16928\n" \
  "v_mfma_f32_16x16x32_bf16 v[228:231], v[116:119], v[132:135], 0\n" \
  "ds_read_b64_tr_b16 v[102:103], v160 offset:17184\n" \
  "s_waitcnt lgkmcnt(4)\n" \
  "v_mfma_f32_16x16x32_bf16 v[200:203], v[120:123], v[128:131], 0\n" \
  "ds_read_b64_tr_b16 v[104:105], v160 offset:16960\n" \
  "v_mfma_f32_16x16x32_bf16 v[232:235], v[120:123], v[132:135], 0\n" \
  "ds_read_b64_tr_b16 v[106:107], v160 offset:17216\n" \
  "v_mfma_f32_16x16x32_bf16 v[204:207], v[124:127], v[128:131], 0\n" \
  "ds_read_b64_tr_b16 v[108:109], v160 offset:16992\n" \
  "v_mfma_f32_16x16x32_bf16 v[236:239], v[124:127], v[132:135], 0\n" \
  "ds_read_b64_tr_b16 v[110:111], v160 offset:17248\n" \
  "s_waitcnt vmcnt(4) lgkmcnt(4)\n" \
  "v_mfma_f32_16x16x32_bf16 v[176:179], v[96:99], v[136:139], v[176:179]\n" \
  "s_setprio 0\n" \
  "ds_read_b64_tr_b16 v[112:113], v160 offset:17024\n" \
  "v_mfma_f32_16x16x32_bf16 v[208:211], v[96:99], v[140:143], v[208:211]\n" \
  "ds_read_b64_tr_b16 v[114:115], v160 offset:17280\n" \
  "v_mfma_f32_16x16x32_bf16 v[180:183], v[100:103], v[136:139], v[180:183]\n" \
  "ds_read_b64_tr_b16 v[116:117], v160 offset:17056\n" \
  "v_mfma_f32_16x16x32_bf16 v[212:215], v[100:103], v[140:143], v[212:215]\n" \
  "ds_read_b64_tr_b16 v[118:119], v160 offset:17312\n" \
  "s_waitcnt lgkmcnt(4)\n" \
  "v_mfma_f32_16x16x32_bf16 v[184:187], v[104:107], v[136:139], v[184:187]\n" \
  "ds_read_b64_tr_b16 v[120:121], v160 offset:17088\n" \
  "v_mfma_f32_16x16x32_bf16 v[216:219], v[104:107], v[140:143], v[216:219]\n" \
  "ds_read_b64_tr_b16 v[122:123], v160 offset:17344\n" \
  "s_mov_b32 s53, 16384\n" \
  "buffer_load_dwordx4 v[128:131], %12, s[40:43], s53 offen offset:0 sc1\n" \
  "v_mfma_f32_16x16x32_bf16 v[188:191], v[108:111], v[136:139], v[188:191]\n" \
  "ds_read_b64_tr_b16 v[124:125], v160 offset:17120\n" \
  "v_mfma_f32_16x16x32_bf16 v[220:223], v[108:111], v[140:143], v[220:223]\n" \
  "ds_read_b64_tr_b16 v[126:127], v160 offset:17376\n" \
  "s_mov_b32 s53, 16384\n" \
  "buffer_load_dwordx4 v[132:135], %18, s[40:43], s53 offen offset:0 sc1\n" \
  "s_waitcnt lgkmcnt(4)\n" \
  "v_mfma_f32_16x16x32_bf16 v[192:195], v[112:115], v[136:139], v[192:195]\n" \
  "v_add_u32 v160, 67584, %13\n" \
  "s_waitcnt vmcnt(18)\n" \
  "s_barrier\n" \
  "s_setprio 3\n" \
  "ds_read_b64_tr_b16 v[96:97], v160 offset:0\n" \
  "v_mfma_f32_16x16x32_bf16 v[224:227], v[112:115], v[140:143], v[224:227]\n" \
  "ds_read_b64_tr_b16 v[98:99], v160 offset:256\n" \
  "v_mfma_f32_16x16x32_bf16 v[196:199], v[116:119], v[136:139], v[196:199]\n" \
  "ds_read_b64_tr_b16 v[100:101], v160 offset:32\n" \
  "v_mfma_f32_16x16x32_bf16 v[228:231], v[116:119], v[140:143], v[228:231]\n" \
  "ds_read_b64_tr_b16 v[102:103], v160 offset:288\n" \
  "s_waitcnt lgkmcnt(4)\n" \
  "v_mfma_f32_16x16x32_bf16 v[200:203], v[120:123], v[136:139], v[200:203]\n" \
  "ds_read_b64_tr_b16 v[104:105], v160 offset:64\n" \
  "v_mfma_f32_16x16x32_bf16 v[232:235], v[120:123], v[140:143], v[232:235]\n" \
  "ds_read_b64_tr_b16 v[106:107], v160 offset:320\n" \
  "v_mfma_f32_16x16x32_bf16 v[204:207], v[124:127], v[136:139], v[204:207]\n" \
  "ds_read_b64_tr_b16 v[108:109], v160 offset:96\n" \
  "v_mfma_f32_16x16x32_bf16 v[236:239], v[124:127], v[140:143], v[236:239]\n" \
  "ds_read_b64_tr_b16 v[110:111], v160 offset:352\n" \
  "s_waitcnt vmcnt(4) lgkmcnt(4)\n" \
  "v_mfma_f32_16x16x32_bf16 v[176:179], v[96:99], v[144:147], v[176:179]\n" \
  "s_setprio 2\n" \
  "ds_read_b64_tr_b16 v[112:113], v160 offset:128\n" \
  "v_mfma_f32_16x16x32_bf16 v[208:211], v[96:99], v[148:151], v[208:211]\n" \
  "ds_read_b64_tr_b16 v[114:115], v160 offset:384\n" \
  "v_mfma_f32_16x16x32_bf16 v[180:183], v[100:103], v[144:147], v[180:183]\n" \
  "ds_read_b64_tr_b16 v[116:117], v160 offset:160\n" \
  "v_mfma_f32_16x16x32_bf16 v[212:215], v[100:103], v[148:151], v[212:215]\n" \
  "ds_read_b64_tr_b16 v[118:119], v160 offset:416\n" \
  "s_waitcnt lgkmcnt(4)\n" \
  "v_mfma_f32_16x16x32_bf16 v[184:187], v[104:107], v[144:147], v[184:187]\n" \
  "ds_read_b64_tr_b16 v[120:121], v160 offset:192\n" \
  "v_mfma_f32_16x16x32_bf16 v[216:219], v[104:107], v[148:151], v[216:219]\n" \
  "ds_read_b64_tr_b16 v[122:123], v160 offset:448\n" \
  "s_mov_b32 s53, 20480\n" \
  "buffer_load_dwordx4 v[136:139], %12, s[40:43], s53 offen offset:0 sc1\n" \
  "v_mfma_f32_16x16x32_bf16 v[188:191], v[108:111], v[144:147], v[188:191]\n" \
  "ds_read_b64_tr_b16 v[124:125], v160 offset:224\n" \
  "v_mfma_f32_16x16x32_bf16 v[220:223], v[108:111], v[148:151], v[220:223]\n" \
  "ds_read_b64_tr_b16 v[126:127], v160 offset:480\n" \
  "s_mov_b32 s53, 20480\n" \
  "buffer_load_dwordx4 v[140:143], %18, s[40:43], s53 offen offset:0 sc1\n" \
  "s_waitcnt lgkmcnt(4)\n" \
  "v_mfma_f32_16x16x32_bf16 v[192:195], v[112:115], v[144:147], v[192:195]\n" \
  "s_setprio 1\n" \
  "ds_read_b64_tr_b16 v[96:97], v160 offset:16896\n" \
  "v_mfma_f32_16x16x32_bf16 v[224:227], v[112:115], v[148:151], v[224:227]\n" \
  "ds_read_b64_tr_b16 v[98:99], v160 offset:17152\n" \
  "v_mfma_f32_16x16x32_bf16 v[196:199], v[116:119], v[144:147], v[196:199]\n" \
  "ds_read_b64_tr_b16 v[100:101], v160 offset:16928\n" \
  "v_mfma_f32_16x16x32_bf16 v[228:231], v[116:119], v[148:151], v[228:231]\n" \
  "ds_read_b64_tr_b16 v[102:103], v160 offset:17184\n" \
  "s_waitcnt lgkmcnt(4)\n" \
  "v_mfma_f32_16x16x32_bf16 v[200:203], v[120:123], v[144:147], v[200:203]\n" \
  "ds_read_b64_tr_b16 v[104:105], v160 offset:16960\n" \
  "v_mfma_f32_16x16x32_bf16 v[232:235], v[120:123], v[148:151], v[232:235]\n" \
  "ds_read_b64_tr_b16 v[106:107], v160 offset:17216\n" \
  "v_mfma_f32_16x16x32_bf16 v[204:207], v[124:127], v[144:147], v[204:207]\n" \
  "ds_read_b64_tr_b16 v[108:109], v160 offset:16992\n" \
  "v_mfma_f32_16x16x32_bf16 v[236:239], v[124:127], v[148:151], v[236:239]\n" \
  "ds_read_b64_tr_b16 v[110:111], v160 offset:17248\n" \
  "s_waitcnt vmcnt(4) lgkmcnt(4)\n" \
  "v_mfma_f32_16x16x32_bf16 v[176:179], v[96:99], v[152:155], v[176:179]\n" \
  "s_setprio 0\n" \
  "ds_read_b64_tr_b16 v[112:113], v160 offset:17024\n" \
  "v_mfma_f32_16x16x32_bf16 v[208:211], v[96:99], v[156:159], v[208:211]\n" \
  "ds_read_b64_tr_b16 v[114:115], v160 offset:17280\n" \
  "v_mfma_f32_16x16x32_bf16 v[180:183], v[100:103], v[152:155], v[180:183]\n" \
  "ds_read_b64_tr_b16 v[116:117], v160 offset:17056\n" \
  "v_mfma_f32_16x16x32_bf16 v[212:215], v[100:103], v[156:159], v[212:215]\n" \
  "ds_read_b64_tr_b16 v[118:119], v160 offset:17312\n" \
  "s_waitcnt lgkmcnt(4)\n" \
  "v_mfma_f32_16x16x32_bf16 v[184:187], v[104:107], v[152:155], v[184:187]\n" \
  "ds_read_b64_tr_b16 v[120:121], v160 offset:17088\n" \
  "v_mfma_f32_16x16x32_bf16 v[216:219], v[104:107], v[156:159], v[216:219]\n" \
  "ds_read_b64_tr_b16 v[122:123], v160 offset:17344\n" \
  "s_mov_b32 s53, 24576\n" \
  "buffer_load_dwordx4 v[144:147], %12, s[40:43], s53 offen offset:0 sc1\n" \
  "v_mfma_f32_16x16x32_bf16 v[188:191], v[108:111], v[152:155], v[188:191]\n" \
  "ds_read_b64_tr_b16 v[124:125], v160 offset:17120\n" \
  "v_mfma_f32_16x16x32_bf16 v[220:223], v[108:111], v[156:159], v[220:223]\n" \
  "ds_read_b64_tr_b16 v[126:127], v160 offset:17376\n" \
  "s_mov_b32 s53, 24576\n" \
  "buffer_load_dwordx4 v[148:151], %18, s[40:43], s53 offen offset:0 sc1\n" \
  "s_waitcnt lgkmcnt(4)\n" \
  "v_mfma_f32_16x16x32_bf16 v[192:195], v[112:115], v[152:155], v[192:195]\n" \
  "v_add_u32 v160, 101376, %13\n" \
  "s_waitcnt vmcnt(18)\n" \
  "s_barrier\n" \
  "s_setprio 3\n" \
  "ds_read_b64_tr_b16 v[96:97], v160 offset:0\n" \
  "v_mfma_f32_16x16x32_bf16 v[224:227], v[112:115], v[156:159], v[224:227]\n" \
  "ds_read_b64_tr_b16 v[98:99], v160 offset:256\n" \
  "v_mfma_f32_16x16x32_bf16 v[196:199], v[116:119], v[152:155], v[196:199]\n" \
  "ds_read_b64_tr_b16 v[100:101], v160 offset:32\n" \
  "v_mfma_f32_16x16x32_bf16 v[228:231], v[116:119], v[156:159], v[228:231]\n" \
  "ds_read_b64_tr_b16 v[102:103], v160 offset:288\n" \
  "s_waitcnt lgkmcnt(4)\n" \
  "v_mfma_f32_16x16x32_bf16 v[200:203], v[120:123], v[152:155], v[200:203]\n" \
  "ds_read_b64_tr_b16 v[104:105], v160 offset:64\n" \
  "v_mfma_f32_16x16x32_bf16 v[232:235], v[120:123], v[156:159], v[232:235]\n" \
  "ds_read_b64_tr_b16 v[106:107], v160 offset:320\n" \
  "v_mfma_f32_16x16x32_bf16 v[204:207], v[124:127], v[152:155], v[204:207]\n" \
  "ds_read_b64_tr_b16 v[108:109], v160 offset:96\n" \
  "v_mfma_f32_16x16x32_bf16 v[236:239], v[124:127], v[156:159], v[236:239]\n" \
  "ds_read_b64_tr_b16 v[110:111], v160 offset:352\n" \
  "s_waitcnt vmcnt(4) lgkmcnt(4)\n" \
  "v_mfma_f32_16x16x32_bf16 v[176:179], v[96:99], v[128:131], v[176:179]\n" \
  "s_setprio 2\n" \
  "ds_read_b64_tr_b16 v[112:113], v160 offset:128\n" \
  "v_mfma_f32_16x16x32_bf16 v[208:211], v[96:99], v[132:135], v[208:211]\n" \
  "ds_read_b64_tr_b16 v[114:115], v160 offset:384\n" \
  "s_add_u32 m0, %10, 33792\n" \
  "s_mov_b32 s53, 1048576\n" \
  "buffer_load_dwordx4 %14, s[44:47], s53 offen lds\n" \
  "v_mfma_f32_16x16x32_bf16 v[180:183], v[100:103], v[128:131], v[180:183]\n" \
  "ds_read_b64_tr_b16 v[116:117], v160 offset:160\n" \
  "v_mfma_f32_16x16x32_bf16 v[212:215], v[100:103], v[132:135], v[212:215]\n" \
  "ds_read_b64_tr_b16 v[118:119], v160 offset:416\n" \
  "s_waitcnt lgkmcnt(4)\n" \
  "v_mfma_f32_16x16x32_bf16 v[184:187], v[104:107], v[128:131], v[184:187]\n" \
  "ds_read_b64_tr_b16 v[120:121], v160 offset:192\n" \
  "v_mfma_f32_16x16x32_bf16 v[216:219], v[104:107], v[132:135], v[216:219]\n" \
  "ds_read_b64_tr_b16 v[122:123], v160 offset:448\n" \
  "s_mov_b32 s53, 28672\n" \
  "buffer_load_dwordx4 v[152:155], %12, s[40:43], s53 offen offset:0 sc1\n" \
  "v_mfma_f32_16x16x32_bf16 v[188:191], v[108:111], v[128:131], v[188:191]\n" \
  "ds_read_b64_tr_b16 v[124:125], v160 offset:224\n" \
  "v_mfma_f32_16x16x32_bf16 v[220:223], v[108:111], v[132:135], v[220:223]\n" \
  "ds_read_b64_tr_b16 v[126:127], v160 offset:480\n" \
  "s_mov_b32 s53, 28672\n" \
  "buffer_load_dwordx4 v[156:159], %18, s[40:43], s53 offen offset:0 sc1\n" \
  "s_waitcnt lgkmcnt(4)\n" \
  "v_mfma_f32_16x16x32_bf16 v[192:195], v[112:115], v[128:131], v[192:195]\n" \
  "s_setprio 1\n" \
  "ds_read_b64_tr_b16 v[96:97], v160 offset:16896\n" \
  "v_mfma_f32_16x16x32_bf16 v[224:227], v[112:115], v[132:135], v[224:227]\n" \
  "ds_read_b64_tr_b16 v[98:99], v160 offset:17152\n" \
  "s_add_u32 m0, %10, 42240\n" \
  "s_mov_b32 s53, 1048576\n" \
  "buffer_load_dwordx4 %15, s[44:47], s53 offen lds\n" \
  "v_mfma_f32_16x16x32_bf16 v[196:199], v[116:119], v[128:131], v[196:199]\n" \
  "ds_read_b64_tr_b16 v[100:101], v160 offset:16928\n" \
  "v_mfma_f32_16x16x32_bf16 v[228:231], v[116:119], v[132:135], v[228:231]\n" \
  "ds_read_b64_tr_b16 v[102:103], v160 offset:17184\n" \
  "s_waitcnt lgkmcnt(4)\n" \
  "v_mfma_f32_16x16x32_bf16 v[200:203], v[120:123], v[128:131], v[200:203]\n" \
  "ds_read_b64_tr_b16 v[104:105], v160 offset:16960\n" \
  "v_mfma_f32_16x16x32_bf16 v[232:235], v[120:123], v[132:135], v[232:235]\n" \
  "ds_read_b64_tr_b16 v[106:107], v160 offset:17216\n" \
  "v_mfma_f32_16x16x32_bf16 v[204:207], v[124:127], v[128:131], v[204:207]\n" \
  "ds_read_b64_tr_b16 v[108:109], v160 offset:16992\n" \
  "v_mfma_f32_16x16x32_bf16 v[236:239], v[124:127], v[132:135], v[236:239]\n" \
  "ds_read_b64_tr_b16 v[110:111], v160 offset:17248\n" \
  "s_waitcnt vmcnt(6) lgkmcnt(4)\n" \
  "v_mfma_f32_16x16x32_bf16 v[176:179], v[96:99], v[136:139], v[176:179]\n" \
  "s_setprio 0\n" \
  "ds_read_b64_tr_b16 v[112:113], v160 offset:17024\n" \
  "v_mfma_f32_16x16x32_bf16 v[208:211], v[96:99], v[140:143], v[208:211]\n" \
  "ds_read_b64_tr_b16 v[114:115], v160 offset:17280\n" \
  "s_add_u32 m0, %10, 50688\n" \
  "s_mov_b32 s53, 1048576\n" \
  "buffer_load_dwordx4 %16, s[44:47], s53 offen lds\n" \
  "v_mfma_f32_16x16x32_bf16 v[180:183], v[100:103], v[136:139], v[180:183]\n" \
  "ds_read_b64_tr_b16 v[116:117], v160 offset:17056\n" \
  "v_mfma_f32_16x16x32_bf16 v[212:215], v[100:103], v[140:143], v[212:215]\n" \
  "ds_read_b64_tr_b16 v[118:119], v160 offset:17312\n" \
  "s_waitcnt lgkmcnt(4)\n" \
  "v_mfma_f32_16x16x32_bf16 v[184:187], v[104:107], v[136:139], v[184:187]\n" \
  "ds_read_b64_tr_b16 v[120:121], v160 offset:17088\n" \
  "v_mfma_f32_16x16x32_bf16 v[216:219], v[104:107], v[140:143], v[216:219]\n" \
  "ds_read_b64_tr_b16 v[122:123], v160 offset:17344\n" \
  "s_mov_b32 s53, 32768\n" \
  "buffer_load_dwordx4 v[128:131], %12, s[40:43], s53 offen offset:0 sc1\n" \
  "v_mfma_f32_16x16x32_bf16 v[188:191], v[108:111], v[136:139], v[188:191]\n" \
  "ds_read_b64_tr_b16 v[124:125], v160 offset:17120\n" \
  "v_mfma_f32_16x16x32_bf16 v[220:223], v[108:111], v[140:143], v[220:223]\n" \
  "ds_read_b64_tr_b16 v[126:127], v160 offset:17376\n" \
  "s_mov_b32 s53, 32768\n" \
  "buffer_load_dwordx4 v[132:135], %18, s[40:43], s53 offen offset:0 sc1\n" \
  "s_waitcnt lgkmcnt(4)\n" \
  "v_mfma_f32_16x16x32_bf16 v[192:195], v[112:115], v[136:139], v[192:195]\n" \
  "v_add_u32 v160, 0, %13\n" \
  "s_waitcnt vmcnt(21)\n" \
  "s_barrier\n" \
  "s_setprio 3\n" \
  "ds_read_b64_tr_b16 v[96:97], v160 offset:0\n" \
  "v_mfma_f32_16x16x32_bf16 v[224:227], v[112:115], v[140:143], v[224:227]\n" \
  "ds_read_b64_tr_b16 v[98:99], v160 offset:256\n" \
  "s_add_u32 m0, %10, 59136\n" \
  "s_mov_b32 s53, 1048576\n" \
  "buffer_load_dwordx4 %17, s[44:47], s53 offen lds\n" \
  "v_mfma_f32_16x16x32_bf16 v[196:199], v[116:119], v[136:139], v[196:199]\n" \
  "ds_read_b64_tr_b16 v[100:101], v160 offset:32\n" \
  "v_mfma_f32_16x16x32_bf16 v[228:231], v[116:119], v[140:143], v[228:231]\n" \
  "ds_read_b64_tr_b16 v[102:103], v160 offset:288\n" \
  "s_waitcnt lgkmcnt(4)\n" \
  "v_mfma_f32_16x16x32_bf16 v[200:203], v[120:123], v[136:139], v[200:203]\n" \
  "ds_read_b64_tr_b16 v[104:105], v160 offset:64\n" \
  "v_mfma_f32_16x16x32_bf16 v[232:235], v[120:123], v[140:143], v[232:235]\n" \
  "ds_read_b64_tr_b16 v[106:107], v160 offset:320\n" \
  "v_mfma_f32_16x16x32_bf16 v[204:207], v[124:127], v[136:139], v[204:207]\n" \
  "ds_read_b64_tr_b16 v[108:109], v160 offset:96\n" \
  "v_mfma_f32_16x16x32_bf16 v[236:239], v[124:127], v[140:143], v[236:239]\n" \
  "ds_read_b64_tr_b16 v[110:111], v160 offset:352\n" \
  "s_waitcnt vmcnt(8) lgkmcnt(4)\n" \
  "v_mfma_f32_16x16x32_bf16 v[176:179], v[96:99], v[144:147], v[176:179]\n" \
  "s_setprio 2\n" \
  "ds_read_b64_tr_b16 v[112:113], v160 offset:128\n" \
  "v_mfma_f32_16x16x32_bf16 v[208:211], v[96:99], v[148:151], v[208:211]\n" \
  "ds_read_b64_tr_b16 v[114:115], v160 offset:384\n" \
  "s_add_u32 m0, %10, 67584\n" \
  "s_mov_b32 s53, 1310720\n" \
  "buffer_load_dwordx4 %14, s[44:47], s53 offen lds\n" \
  "v_mfma_f32_16x16x32_bf16 v[180:183], v[100:103], v[144:147], v[180:183]\n" \
  "ds_read_b64_tr_b16 v[116:117], v160 offset:160\n" \
  "v_mfma_f32_16x16x32_bf16 v[212:215], v[100:103], v[148:151], v[212:215]\n" \
  "ds_read_b64_tr_b16 v[118:119], v160 offset:416\n" \
  "s_waitcnt lgkmcnt(4)\n" \
  "v_mfma_f32_16x16x32_bf16 v[184:187], v[104:107], v[144:147], v[184:187]\n" \
  "ds_read_b64_tr_b16 v[120:121], v160 offset:192\n" \
  "v_mfma_f32_16x16x32_bf16 v[216:219], v[104:107], v[148:151], v[216:219]\n" \
  "ds_read_b64_tr_b16 v[122:123], v160 offset:448\n" \
  "s_mov_b32 s53, 36864\n" \
  "buffer_load_dwordx4 v[136:139], %12, s[40:43], s53 offen offset:0 sc1\n" \
  "v_mfma_f32_16x16x32_bf16 v[188:191], v[108:111], v[144:147], v[188:191]\n" \
  "ds_read_b64_tr_b16 v[124:125], v160 offset:224\n" \
  "v_mfma_f32_16x16x32_bf16 v[220:223], v[108:111], v[148:151], v[220:223]\n" \
  "ds_read_b64_tr_b16 v[126:127], v160 offset:480\n" \
  "s_mov_b32 s53, 36864\n" \
  "buffer_load_dwordx4 v[140:143], %18, s[40:43], s53 offen offset:0 sc1\n" \
  "s_waitcnt lgkmcnt(4)\n" \
  "v_mfma_f32_16x16x32_bf16 v[192:195], v[112:115], v[144:147], v[192:195]\n" \
  "s_setprio 1\n" \
  "ds_read_b64_tr_b16 v[96:97], v160 offset:16896\n" \
  "v_mfma_f32_16x16x32_bf16 v[224:227], v[112:115], v[148:151], v[224:227]\n" \
  "ds_read_b64_tr_b16 v[98:99], v160 offset:17152\n" \
  "s_add_u32 m0, %10, 76032\n" \
  "s_mov_b32 s53, 1310720\n" \
  "buffer_load_dwordx4 %15, s[44:47], s53 offen lds\n" \
  "v_mfma_f32_16x16x32_bf16 v[196:199], v[116:119], v[144:147], v[196:199]\n" \
  "ds_read_b64_tr_b16 v[100:101], v160 offset:16928\n" \
  "v_mfma_f32_16x16x32_bf16 v[228:231], v[116:119], v[148:151], v[228:231]\n" \
  "ds_read_b64_tr_b16 v[102:103], v160 offset:17184\n" \
  "s_waitcnt lgkmcnt(4)\n" \
  "v_mfma_f32_16x16x32_bf16 v[200:203], v[120:123], v[144:147], v[200:203]\n" \
  "ds_read_b64_tr_b16 v[104:105], v160 offset:16960\n" \
  "v_mfma_f32_16x16x32_bf16 v[232:235], v[120:123], v[148:151], v[232:235]\n" \
  "ds_read_b64_tr_b16 v[106:107], v160 offset:17216\n" \
  "v_mfma_f32_16x16x32_bf16 v[204:207], v[124:127], v[144:147], v[204:207]\n" \
  "ds_read_b64_tr_b16 v[108:109], v160 offset:16992\n" \
  "v_mfma_f32_16x16x32_bf16 v[236:239], v[124:127], v[148:151], v[236:239]\n" \
  "ds_read_b64_tr_b16 v[110:111], v160 offset:17248\n" \
  "s_waitcnt vmcnt(9) lgkmcnt(4)\n" \
  "v_mfma_f32_16x16x32_bf16 v[176:179], v[96:99], v[152:155], v[176:179]\n" \
  "s_setprio 0\n" \
  "ds_read_b64_tr_b16 v[112:113], v160 offset:17024\n" \
  "v_mfma_f32_16x16x32_bf16 v[208:211], v[96:99], v[156:159], v[208:211]\n" \
  "ds_read_b64_tr_b16 v[114:115], v160 offset:17280\n" \
  "s_add_u32 m0, %10, 84480\n" \
  "s_mov_b32 s53, 1310720\n" \
  "buffer_load_dwordx4 %16, s[44:47], s53 offen lds\n" \
  "v_mfma_f32_16x16x32_bf16 v[180:183], v[100:103], v[152:155], v[180:183]\n" \
  "ds_read_b64_tr_b16 v[116:117], v160 offset:17056\n" \
  "v_mfma_f32_16x16x32_bf16 v[212:215], v[100:103], v[156:159], v[212:215]\n" \
  "ds_read_b64_tr_b16 v[118:119], v160 offset:17312\n" \
  "s_waitcnt lgkmcnt(4)\n" \
  "v_mfma_f32_16x16x32_bf16 v[184:187], v[104:107], v[152:155], v[184:187]\n" \
  "ds_read_b64_tr_b16 v[120:121], v160 offset:17088\n" \
  "v_mfma_f32_16x16x32_bf16 v[216:219], v[104:107], v[156:159], v[216:219]\n" \
  "ds_read_b64_tr_b16 v[122:123], v160 offset:17344\n" \
  "s_mov_b32 s53, 40960\n" \
  "buffer_load_dwordx4 v[144:147], %12, s[40:43], s53 offen offset:0 sc1\n" \
  "v_mfma_f32_16x16x32_bf16 v[188:191], v[108:111], v[152:155], v[188:191]\n" \
  "ds_read_b64_tr_b16 v[124:125], v160 offset:17120\n" \
  "v_mfma_f32_16x16x32_bf16 v[220:223], v[108:111], v[156:159], v[220:223]\n" \
  "ds_read_b64_tr_b16 v[126:127], v160 offset:17376\n" \
  "s_mov_b32 s53, 40960\n" \
  "buffer_load_dwordx4 v[148:151], %18, s[40:43], s53 offen offset:0 sc1\n" \
  "s_waitcnt lgkmcnt(4)\n" \
  "v_mfma_f32_16x16x32_bf16 v[192:195], v[112:115], v[152:155], v[192:195]\n" \
  "v_add_u32 v160, 33792, %13\n" \
  "s_waitcnt vmcnt(7)\n" \
  "s_barrier\n" \
  "s_setprio 3\n" \
  "ds_read_b64_tr_b16 v[96:97], v160 offset:0\n" \
  "v_mfma_f32_16x16x32_bf16 v[224:227], v[112:115], v[156:159], v[224:227]\n" \
  "ds_read_b64_tr_b16 v[98:99], v160 offset:256\n" \
  "s_add_u32 m0, %10, 92928\n" \
  "s_mov_b32 s53, 1310720\n" \
  "buffer_load_dwordx4 %17, s[44:47], s53 offen lds\n" \
  "v_mfma_f32_16x16x32_bf16 v[196:199], v[116:119], v[152:155], v[196:199]\n" \
  "ds_read_b64_tr_b16 v[100:101], v160 offset:32\n" \
  "v_mfma_f32_16x16x32_bf16 v[228:231], v[116:119], v[156:159], v[228:231]\n" \
  "ds_read_b64_tr_b16 v[102:103], v160 offset:288\n" \
  "s_waitcnt lgkmcnt(4)\n" \
  "v_mfma_f32_16x16x32_bf16 v[200:203], v[120:123], v[152:155], v[200:203]\n" \
  "ds_read_b64_tr_b16 v[104:105], v160 offset:64\n" \
  "v_mfma_f32_16x16x32_bf16 v[232:235], v[120:123], v[156:159], v[232:235]\n" \
  "ds_read_b64_tr_b16 v[106:107], v160 offset:320\n" \
  "v_mfma_f32_16x16x32_bf16 v[204:207], v[124:127], v[152:155], v[204:207]\n" \
  "ds_read_b64_tr_b16 v[108:109], v160 offset:96\n" \
  "v_mfma_f32_16x16x32_bf16 v[236:239], v[124:127], v[156:159], v[236:239]\n" \
  "ds_read_b64_tr_b16 v[110:111], v160 offset:352\n" \
  "Lloop_%=:\n" \
  "s_waitcnt vmcnt(9) lgkmcnt(4)\n" \
  "v_mfma_f32_16x16x32_bf16 v[176:179], v[96:99], v[128:131], v[176:179]\n" \
  "s_setprio 2\n" \
  "ds_read_b64_tr_b16 v[112:113], v160 offset:128\n" \
  "v_mfma_f32_16x16x32_bf16 v[208:211], v[96:99], v[132:135], v[208:211]\n" \
  "ds_read_b64_tr_b16 v[114:115], v160 offset:384\n" \
  "s_add_u32 m0, s51, 0\n" \
  "s_nop 0\n" \
  "buffer_load_dwordx4 %14, s[44:47], s49 offen lds\n" \
  "v_mfma_f32_16x16x32_bf16 v[180:183], v[100:103], v[128:131], v[180:183]\n" \
  "ds_read_b64_tr_b16 v[116:117], v160 offset:160\n" \
  "v_mfma_f32_16x16x32_bf16 v[212:215], v[100:103], v[132:135], v[212:215]\n" \
  "ds_read_b64_tr_b16 v[118:119], v160 offset:416\n" \
  "s_waitcnt lgkmcnt(4)\n" \
  "v_mfma_f32_16x16x32_bf16 v[184:187], v[104:107], v[128:131], v[184:187]\n" \
  "ds_read_b64_tr_b16 v[120:121], v160 offset:192\n" \
  "v_mfma_f32_16x16x32_bf16 v[216:219], v[104:107], v[132:135], v[216:219]\n" \
  "ds_read_b64_tr_b16 v[122:123], v160 offset:448\n" \
  "s_add_u32 s53, s48, 4096\n" \
  "buffer_load_dwordx4 v[152:155], %12, s[40:43], s53 offen offset:0 sc1\n" \
  "v_mfma_f32_16x16x32_bf16 v[188:191], v[108:111], v[128:131], v[188:191]\n" \
  "ds_read_b64_tr_b16 v[124:125], v160 offset:224\n" \
  "v_mfma_f32_16x16x32_bf16 v[220:223], v[108:111], v[132:135], v[220:223]\n" \
  "ds_read_b64_tr_b16 v[126:127], v160 offset:480\n" \
  "s_add_u32 s53, s48, 4096\n" \
  "buffer_load_dwordx4 v[156:159], %18, s[40:43], s53 offen offset:0 sc1\n" \
  "s_add_u32 s48, s48, 8192\n" \
  "s_waitcnt lgkmcnt(4)\n" \
  "v_mfma_f32_16x16x32_bf16 v[192:195], v[112:115], v[128:131], v[192:195]\n" \
  "s_setprio 1\n" \
  "ds_read_b64_tr_b16 v[96:97], v160 offset:16896\n" \
  "v_mfma_f32_16x16x32_bf16 v[224:227], v[112:115], v[132:135], v[224:227]\n" \
  "ds_read_b64_tr_b16 v[98:99], v160 offset:17152\n" \
  "s_add_u32 m0, s51, 8448\n" \
  "s_nop 0\n" \
  "buffer_load_dwordx4 %15, s[44:47], s49 offen lds\n" \
  "v_mfma_f32_16x16x32_bf16 v[196:199], v[116:119], v[128:131], v[196:199]\n" \
  "ds_read_b64_tr_b16 v[100:101], v160 offset:16928\n" \
  "v_mfma_f32_16x16x32_bf16 v[228:231], v[116:119], v[132:135], v[228:231]\n" \
  "ds_read_b64_tr_b16 v[102:103], v160 offset:17184\n" \
  "s_waitcnt lgkmcnt(4)\n" \
  "v_mfma_f32_16x16x32_bf16 v[200:203], v[120:123], v[128:131], v[200:203]\n" \
  "ds_read_b64_tr_b16 v[104:105], v160 offset:16960\n" \
  "v_mfma_f32_16x16x32_bf16 v[232:235], v[120:123], v[132:135], v[232:235]\n" \
  "ds_read_b64_tr_b16 v[106:107], v160 offset:17216\n" \
  "v_mfma_f32_16x16x32_bf16 v[204:207], v[124:127], v[128:131], v[204:207]\n" \
  "ds_read_b64_tr_b16 v[108:109], v160 offset:16992\n" \
  "v_mfma_f32_16x16x32_bf16 v[236:239], v[124:127], v[132:135], v[236:239]\n" \
  "ds_read_b64_tr_b16 v[110:111], v160 offset:17248\n" \
  "s_waitcnt vmcnt(9) lgkmcnt(4)\n" \
  "v_mfma_f32_16x16x32_bf16 v[176:179], v[96:99], v[136:139], v[176:179]\n" \
  "s_setprio 0\n" \
  "ds_read_b64_tr_b16 v[112:113], v160 offset:17024\n" \
  "v_mfma_f32_16x16x32_bf16 v[208:211], v[96:99], v[140:143], v[208:211]\n" \
  "ds_read_b64_tr_b16 v[114:115], v160 offset:17280\n" \
  "s_add_u32 m0, s51, 16896\n" \
  "s_nop 0\n" \
  "buffer_load_dwordx4 %16, s[44:47], s49 offen lds\n" \
  "v_mfma_f32_16x16x32_bf16 v[180:183], v[100:103], v[136:139], v[180:183]\n" \
  "ds_read_b64_tr_b16 v[116:117], v160 offset:17056\n" \
  "v_mfma_f32_16x16x32_bf16 v[212:215], v[100:103], v[140:143], v[212:215]\n" \
  "ds_read_b64_tr_b16 v[118:119], v160 offset:17312\n" \
  "s_waitcnt lgkmcnt(4)\n" \
  "v_mfma_f32_16x16x32_bf16 v[184:187], v[104:107], v[136:139], v[184:187]\n" \
  "ds_read_b64_tr_b16 v[120:121], v160 offset:17088\n" \
  "v_mfma_f32_16x16x32_bf16 v[216:219], v[104:107], v[140:143], v[216:219]\n" \
  "ds_read_b64_tr_b16 v[122:123], v160 offset:17344\n" \
  "buffer_load_dwordx4 v[128:131], %12, s[40:43], s48 offen offset:0 sc1\n" \
  "v_mfma_f32_16x16x32_bf16 v[188:191], v[108:111], v[136:139], v[188:191]\n" \
  "ds_read_b64_tr_b16 v[124:125], v160 offset:17120\n" \
  "v_mfma_f32_16x16x32_bf16 v[220:223], v[108:111], v[140:143], v[220:223]\n" \
  "ds_read_b64_tr_b16 v[126:127], v160 offset:17376\n" \
  "buffer_load_dwordx4 v[132:135], %18, s[40:43], s48 offen offset:0 sc1\n" \
  "s_waitcnt lgkmcnt(4)\n" \
  "v_mfma_f32_16x16x32_bf16 v[192:195], v[112:115], v[136:139], v[192:195]\n" \
  "s_add_u32 s50, s50, 33792\n" \
  "s_cmp_eq_u32 s50, 135168\n" \
  "s_cselect_b32 s50, 0, s50\n" \
  "v_add_u32 v160, s50, %13\n" \
  "s_waitcnt vmcnt(7)\n" \
  "s_barrier\n" \
  "s_setprio 3\n" \
  "ds_read_b64_tr_b16 v[96:97], v160 offset:0\n" \
  "v_mfma_f32_16x16x32_bf16 v[224:227], v[112:115], v[140:143], v[224:227]\n" \
  "ds_read_b64_tr_b16 v[98:99], v160 offset:256\n" \
  "s_add_u32 m0, s51, 25344\n" \
  "s_nop 0\n" \
  "buffer_load_dwordx4 %17, s[44:47], s49 offen lds\n" \
  "s_add_u32 s49, s49, 262144\n" \
  "s_add_u32 s51, s51, 33792\n" \
  "s_sub_u32 s53, s51, %10\n" \
  "s_cmp_eq_u32 s53, 135168\n" \
  "s_cselect_b32 s51, %10, s51\n" \
  "v_mfma_f32_16x16x32_bf16 v[196:199], v[116:119], v[136:139], v[196:199]\n" \
  "ds_read_b64_tr_b16 v[100:101], v160 offset:32\n" \
  "v_mfma_f32_16x16x32_bf16 v[228:231], v[116:119], v[140:143], v[228:231]\n" \
  "ds_read_b64_tr_b16 v[102:103], v160 offset:288\n" \
  "s_waitcnt lgkmcnt(4)\n" \
  "v_mfma_f32_16x16x32_bf16 v[200:203], v[120:123], v[136:139], v[200:203]\n" \
  "ds_read_b64_tr_b16 v[104:105], v160 offset:64\n" \
  "v_mfma_f32_16x16x32_bf16 v[232:235], v[120:123], v[140:143], v[232:235]\n" \
  "ds_read_b64_tr_b16 v[106:107], v160 offset:320\n" \
  "v_mfma_f32_16x16x32_bf16 v[204:207], v[124:127], v[136:139], v[204:207]\n" \
  "ds_read_b64_tr_b16 v[108:109], v160 offset:96\n" \
  "v_mfma_f32_16x16x32_bf16 v[236:239], v[124:127], v[140:143], v[236:239]\n" \
  "ds_read_b64_tr_b16 v[110:111], v160 offset:352\n" \
  "s_waitcnt vmcnt(9) lgkmcnt(4)\n" \
  "v_mfma_f32_16x16x32_bf16 v[176:179], v[96:99], v[144:147], v[176:179]\n" \
  "s_setprio 2\n" \
  "ds_read_b64_tr_b16 v[112:113], v160 offset:128\n" \
  "v_mfma_f32_16x16x32_bf16 v[208:211], v[96:99], v[148:151], v[208:211]\n" \
  "ds_read_b64_tr_b16 v[114:115], v160 offset:384\n" \
  "s_add_u32 m0, s51, 0\n" \
  "s_nop 0\n" \
  "buffer_load_dwordx4 %14, s[44:47], s49 offen lds\n" \
  "v_mfma_f32_16x16x32_bf16 v[180:183], v[100:103], v[144:147], v[180:183]\n" \
  "ds_read_b64_tr_b16 v[116:117], v160 offset:160\n" \
  "v_mfma_f32_16x16x32_bf16 v[212:215], v[100:103], v[148:151], v[212:215]\n" \
  "ds_read_b64_tr_b16 v[118:119], v160 offset:416\n" \
  "s_waitcnt lgkmcnt(4)\n" \
  "v_mfma_f32_16x16x32_bf16 v[184:187], v[104:107], v[144:147], v[184:187]\n" \
  "ds_read_b64_tr_b16 v[120:121], v160 offset:192\n" \
  "v_mfma_f32_16x16x32_bf16 v[216:219], v[104:107], v[148:151], v[216:219]\n" \
  "ds_read_b64_tr_b16 v[122:123], v160 offset:448\n" \
  "s_add_u32 s53, s48, 4096\n" \
  "buffer_load_dwordx4 v[136:139], %12, s[40:43], s53 offen offset:0 sc1\n" \
  "v_mfma_f32_16x16x32_bf16 v[188:191], v[108:111], v[144:147], v[188:191]\n" \
  "ds_read_b64_tr_b16 v[124:125], v160 offset:224\n" \
  "v_mfma_f32_16x16x32_bf16 v[220:223], v[108:111], v[148:151], v[220:223]\n" \
  "ds_read_b64_tr_b16 v[126:127], v160 offset:480\n" \
  "s_add_u32 s53, s48, 4096\n" \
  "buffer_load_dwordx4 v[140:143], %18, s[40:43], s53 offen offset:0 sc1\n" \
  "s_add_u32 s48, s48, 8192\n" \
  "s_waitcnt lgkmcnt(4)\n" \
  "v_mfma_f32_16x16x32_bf16 v[192:195], v[112:115], v[144:147], v[192:195]\n" \
  "s_setprio 1\n" \
  "ds_read_b64_tr_b16 v[96:97], v160 offset:16896\n" \
  "v_mfma_f32_16x16x32_bf16 v[224:227], v[112:115], v[148:151], v[224:227]\n" \
  "ds_read_b64_tr_b16 v[98:99], v160 offset:17152\n" \
  "s_add_u32 m0, s51, 8448\n" \
  "s_nop 0\n" \
  "buffer_load_dwordx4 %15, s[44:47], s49 offen lds\n" \
  "v_mfma_f32_16x16x32_bf16 v[196:199], v[116:119], v[144:147], v[196:199]\n" \
  "ds_read_b64_tr_b16 v[100:101], v160 offset:16928\n" \
  "v_mfma_f32_16x16x32_bf16 v[228:231], v[116:119], v[148:151], v[228:231]\n" \
  "ds_read_b64_tr_b16 v[102:103], v160 offset:17184\n" \
  "s_waitcnt lgkmcnt(4)\n" \
  "v_mfma_f32_16x16x32_bf16 v[200:203], v[120:123], v[144:147], v[200:203]\n" \
  "ds_read_b64_tr_b16 v[104:105], v160 offset:16960\n" \
  "v_mfma_f32_16x16x32_bf16 v[232:235], v[120:123], v[148:151], v[232:235]\n" \
  "ds_read_b64_tr_b16 v[106:107], v160 offset:17216\n" \
  "v_mfma_f32_16x16x32_bf16 v[204:207], v[124:127], v[144:147], v[204:207]\n" \
  "ds_read_b64_tr_b16 v[108:109], v160 offset:16992\n" \
  "v_mfma_f32_16x16x32_bf16 v[236:239], v[124:127], v[148:151], v[236:239]\n" \
  "ds_read_b64_tr_b16 v[110:111], v160 offset:17248\n" \
  "s_waitcnt vmcnt(9) lgkmcnt(4)\n" \
  "v_mfma_f32_16x16x32_bf16 v[176:179], v[96:99], v[152:155], v[176:179]\n" \
  "s_setprio 0\n" \
  "ds_read_b64_tr_b16 v[112:113], v160 offset:17024\n" \
  "v_mfma_f32_16x16x32_bf16 v[208:211], v[96:99], v[156:159], v[208:211]\n" \
  "ds_read_b64_tr_b16 v[114:115], v160 offset:17280\n" \
  "s_add_u32 m0, s51, 16896\n" \
  "s_nop 0\n" \
  "buffer_load_dwordx4 %16, s[44:47], s49 offen lds\n" \
  "v_mfma_f32_16x16x32_bf16 v[180:183], v[100:103], v[152:155], v[180:183]\n" \
  "ds_read_b64_tr_b16 v[116:117], v160 offset:17056\n" \
  "v_mfma_f32_16x16x32_bf16 v[212:215], v[100:103], v[156:159], v[212:215]\n" \
  "ds_read_b64_tr_b16 v[118:119], v160 offset:17312\n" \
  "s_waitcnt lgkmcnt(4)\n" \
  "v_mfma_f32_16x16x32_bf16 v[184:187], v[104:107], v[152:155], v[184:187]\n" \
  "ds_read_b64_tr_b16 v[120:121], v160 offset:17088\n" \
  "v_mfma_f32_16x16x32_bf16 v[216:219], v[104:107], v[156:159], v[216:219]\n" \
  "ds_read_b64_tr_b16 v[122:123], v160 offset:17344\n" \
  "buffer_load_dwordx4 v[144:147], %12, s[40:43], s48 offen offset:0 sc1\n" \
  "v_mfma_f32_16x16x32_bf16 v[188:191], v[108:111], v[152:155], v[188:191]\n" \
  "ds_read_b64_tr_b16 v[124:125], v160 offset:17120\n" \
  "v_mfma_f32_16x16x32_bf16 v[220:223], v[108:111], v[156:159], v[220:223]\n" \
  "ds_read_b64_tr_b16 v[126:127], v160 offset:17376\n" \
  "buffer_load_dwordx4 v[148:151], %18, s[40:43], s48 offen offset:0 sc1\n" \
  "s_waitcnt lgkmcnt(4)\n" \
  "v_mfma_f32_16x16x32_bf16 v[192:195], v[112:115], v[152:155], v[192:195]\n" \
  "s_add_u32 s50, s50, 33792\n" \
  "s_cmp_eq_u32 s50, 135168\n" \
  "s_cselect_b32 s50, 0, s50\n" \
  "v_add_u32 v160, s50, %13\n" \
  "s_waitcnt vmcnt(7)\n" \
  "s_barrier\n" \
  "s_setprio 3\n" \
  "ds_read_b64_tr_b16 v[96:97], v160 offset:0\n" \
  "v_mfma_f32_16x16x32_bf16 v[224:227], v[112:115], v[156:159], v[224:227]\n" \
  "ds_read_b64_tr_b16 v[98:99], v160 offset:256\n" \
  "s_add_u32 m0, s51, 25344\n" \
  "s_nop 0\n" \
  "buffer_load_dwordx4 %17, s[44:47], s49 offen lds\n" \
  "s_add_u32 s49, s49, 262144\n" \
  "s_add_u32 s51, s51, 33792\n" \
  "s_sub_u32 s53, s51, %10\n" \
  "s_cmp_eq_u32 s53, 135168\n" \
  "s_cselect_b32 s51, %10, s51\n" \
  "v_mfma_f32_16x16x32_bf16 v[196:199], v[116:119], v[152:155], v[196:199]\n" \
  "ds_read_b64_tr_b16 v[100:101], v160 offset:32\n" \
  "v_mfma_f32_16x16x32_bf16 v[228:231], v[116:119], v[156:159], v[228:231]\n" \
  "ds_read_b64_tr_b16 v[102:103], v160 offset:288\n" \
  "s_waitcnt lgkmcnt(4)\n" \
  "v_mfma_f32_16x16x32_bf16 v[200:203], v[120:123], v[152:155], v[200:203]\n" \
  "ds_read_b64_tr_b16 v[104:105], v160 offset:64\n" \
  "v_mfma_f32_16x16x32_bf16 v[232:235], v[120:123], v[156:159], v[232:235]\n" \
  "ds_read_b64_tr_b16 v[106:107], v160 offset:320\n" \
  "v_mfma_f32_16x16x32_bf16 v[204:207], v[124:127], v[152:155], v[204:207]\n" \
  "ds_read_b64_tr_b16 v[108:109], v160 offset:96\n" \
  "v_mfma_f32_16x16x32_bf16 v[236:239], v[124:127], v[156:159], v[236:239]\n" \
  "ds_read_b64_tr_b16 v[110:111], v160 offset:352\n" \
  "s_sub_u32 s52, s52, 1\n" \
  "s_cmp_lg_u32 s52, 0\n" \
  "s_cbranch_scc1 Lloop_%=\n" \
  "s_waitcnt vmcnt(9) lgkmcnt(6)\n" \
  "v_mfma_f32_16x16x32_bf16 v[176:179], v[96:99], v[128:131], v[176:179]\n" \
  "ds_read_b64_tr_b16 v[112:113], v160 offset:128\n" \
  "v_mfma_f32_16x16x32_bf16 v[208:211], v[96:99], v[132:135], v[208:211]\n" \
  "ds_read_b64_tr_b16 v[114:115], v160 offset:384\n" \
  "s_waitcnt lgkmcnt(6)\n" \
  "v_mfma_f32_16x16x32_bf16 v[180:183], v[100:103], v[128:131], v[180:183]\n" \
  "ds_read_b64_tr_b16 v[116:117], v160 offset:160\n" \
  "v_mfma_f32_16x16x32_bf16 v[212:215], v[100:103], v[132:135], v[212:215]\n" \
  "ds_read_b64_tr_b16 v[118:119], v160 offset:416\n" \
  "s_waitcnt lgkmcnt(6)\n" \
  "v_mfma_f32_16x16x32_bf16 v[184:187], v[104:107], v[128:131], v[184:187]\n" \
  "ds_read_b64_tr_b16 v[120:121], v160 offset:192\n" \
  "v_mfma_f32_16x16x32_bf16 v[216:219], v[104:107], v[132:135], v[216:219]\n" \
  "ds_read_b64_tr_b16 v[122:123], v160 offset:448\n" \
  "s_mov_b32 s53, 110592\n" \
  "buffer_load_dwordx4 v[152:155], %12, s[40:43], s53 offen offset:0 sc1\n" \
  "s_waitcnt lgkmcnt(6)\n" \
  "v_mfma_f32_16x16x32_bf16 v[188:191], v[108:111], v[128:131], v[188:191]\n" \
  "ds_read_b64_tr_b16 v[124:125], v160 offset:224\n" \
  "v_mfma_f32_16x16x32_bf16 v[220:223], v[108:111], v[132:135], v[220:223]\n" \
  "ds_read_b64_tr_b16 v[126:127], v160 offset:480\n" \
  "s_mov_b32 s53, 110592\n" \
  "buffer_load_dwordx4 v[156:159], %18, s[40:43], s53 offen offset:0 sc1\n" \
  "s_waitcnt lgkmcnt(6)\n" \
  "v_mfma_f32_16x16x32_bf16 v[192:195], v[112:115], v[128:131], v[192:195]\n" \
  "ds_read_b64_tr_b16 v[96:97], v160 offset:17024\n" \
  "v_mfma_f32_16x16x32_bf16 v[224:227], v[112:115], v[132:135], v[224:227]\n" \
  "ds_read_b64_tr_b16 v[98:99], v160 offset:17280\n" \
  "s_waitcnt lgkmcnt(6)\n" \
  "v_mfma_f32_16x16x32_bf16 v[196:199], v[116:119], v[128:131], v[196:199]\n" \
  "ds_read_b64_tr_b16 v[100:101], v160 offset:17056\n" \
  "v_mfma_f32_16x16x32_bf16 v[228:231], v[116:119], v[132:135], v[228:231]\n" \
  "ds_read_b64_tr_b16 v[102:103], v160 offset:17312\n" \
  "s_waitcnt lgkmcnt(6)\n" \
  "v_mfma_f32_16x16x32_bf16 v[200:203], v[120:123], v[128:131], v[200:203]\n" \
  "ds_read_b64_tr_b16 v[104:105], v160 offset:17088\n" \
  "v_mfma_f32_16x16x32_bf16 v[232:235], v[120:123], v[132:135], v[232:235]\n" \
  "ds_read_b64_tr_b16 v[106:107], v160 offset:17344\n" \
  "s_waitcnt lgkmcnt(6)\n" \
  "v_mfma_f32_16x16x32_bf16 v[204:207], v[124:127], v[128:131], v[204:207]\n" \
  "ds_read_b64_tr_b16 v[108:109], v160 offset:17120\n" \
  "v_mfma_f32_16x16x32_bf16 v[236:239], v[124:127], v[132:135], v[236:239]\n" \
  "ds_read_b64_tr_b16 v[110:111], v160 offset:17376\n" \
  "s_waitcnt vmcnt(7) lgkmcnt(6)\n" \
  "v_mfma_f32_16x16x32_bf16 v[192:195], v[96:99], v[136:139], v[192:195]\n" \
  "v_add_u32 v160, 67584, %13\n" \
  "s_waitcnt vmcnt(2)\n" \
  "s_barrier\n" \
  "s_setprio 0\n" \
  "ds_read_b64_tr_b16 v[112:113], v160 offset:128\n" \
  "v_mfma_f32_16x16x32_bf16 v[224:227], v[96:99], v[140:143], v[224:227]\n" \
  "ds_read_b64_tr_b16 v[114:115], v160 offset:384\n" \
  "s_waitcnt lgkmcnt(6)\n" \
  "v_mfma_f32_16x16x32_bf16 v[196:199], v[100:103], v[136:139], v[196:199]\n" \
  "ds_read_b64_tr_b16 v[116:117], v160 offset:160\n" \
  "v_mfma_f32_16x16x32_bf16 v[228:231], v[100:103], v[140:143], v[228:231]\n" \
  "ds_read_b64_tr_b16 v[118:119], v160 offset:416\n" \
  "s_waitcnt lgkmcnt(6)\n" \
  "v_mfma_f32_16x16x32_bf16 v[200:203], v[104:107], v[136:139], v[200:203]\n" \
  "ds_read_b64_tr_b16 v[120:121], v160 offset:192\n" \
  "v_mfma_f32_16x16x32_bf16 v[232:235], v[104:107], v[140:143], v[232:235]\n" \
  "ds_read_b64_tr_b16 v[122:123], v160 offset:448\n" \
  "s_waitcnt lgkmcnt(6)\n" \
  "v_mfma_f32_16x16x32_bf16 v[204:207], v[108:111], v[136:139], v[204:207]\n" \
  "ds_read_b64_tr_b16 v[124:125], v160 offset:224\n" \
  "v_mfma_f32_16x16x32_bf16 v[236:239], v[108:111], v[140:143], v[236:239]\n" \
  "ds_read_b64_tr_b16 v[126:127], v160 offset:480\n" \
  "s_waitcnt vmcnt(3) lgkmcnt(6)\n" \
  "v_mfma_f32_16x16x32_bf16 v[192:195], v[112:115], v[144:147], v[192:195]\n" \
  "ds_read_b64_tr_b16 v[96:97], v160 offset:17024\n" \
  "v_mfma_f32_16x16x32_bf16 v[224:227], v[112:115], v[148:151], v[224:227]\n" \
  "ds_read_b64_tr_b16 v[98:99], v160 offset:17280\n" \
  "s_waitcnt lgkmcnt(6)\n" \
  "v_mfma_f32_16x16x32_bf16 v[196:199], v[116:119], v[144:147], v[196:199]\n" \
  "ds_read_b64_tr_b16 v[100:101], v160 offset:17056\n" \
  "v_mfma_f32_16x16x32_bf16 v[228:231], v[116:119], v[148:151], v[228:231]\n" \
  "ds_read_b64_tr_b16 v[102:103], v160 offset:17312\n" \
  "s_waitcnt lgkmcnt(6)\n" \
  "v_mfma_f32_16x16x32_bf16 v[200:203], v[120:123], v[144:147], v[200:203]\n" \
  "ds_read_b64_tr_b16 v[104:105], v160 offset:17088\n" \
  "v_mfma_f32_16x16x32_bf16 v[232:235], v[120:123], v[148:151], v[232:235]\n" \
  "ds_read_b64_tr_b16 v[106:107], v160 offset:17344\n" \
  "s_waitcnt lgkmcnt(6)\n" \
  "v_mfma_f32_16x16x32_bf16 v[204:207], v[124:127], v[144:147], v[204:207]\n" \
  "ds_read_b64_tr_b16 v[108:109], v160 offset:17120\n" \
  "v_mfma_f32_16x16x32_bf16 v[236:239], v[124:127], v[148:151], v[236:239]\n" \
  "ds_read_b64_tr_b16 v[110:111], v160 offset:17376\n" \
  "s_waitcnt vmcnt(0) lgkmcnt(6)\n" \
  "v_mfma_f32_16x16x32_bf16 v[192:195], v[96:99], v[152:155], v[192:195]\n" \
  "v_mfma_f32_16x16x32_bf16 v[224:227], v[96:99], v[156:159], v[224:227]\n" \
  "s_waitcnt lgkmcnt(4)\n" \
  "v_mfma_f32_16x16x32_bf16 v[196:199], v[100:103], v[152:155], v[196:199]\n" \
  "v_mfma_f32_16x16x32_bf16 v[228:231], v[100:103], v[156:159], v[228:231]\n" \
  "s_waitcnt lgkmcnt(2)\n" \
  "v_mfma_f32_16x16x32_bf16 v[200:203], v[104:107], v[152:155], v[200:203]\n" \
  "v_mfma_f32_16x16x32_bf16 v[232:235], v[104:107], v[156:159], v[232:235]\n" \
  "s_waitcnt lgkmcnt(0)\n" \
  "v_mfma_f32_16x16x32_bf16 v[204:207], v[108:111], v[152:155], v[204:207]\n" \
  "v_mfma_f32_16x16x32_bf16 v[236:239], v[108:111], v[156:159], v[236:239]\n" \
  "s_nop 15\n"
#define K5_CLOB "v96", "v97", "v98", "v99", "v100", "v101", "v102", "v103", "v104", "v105", "v106", "v107", "v108", "v109", "v110", "v111", "v112", "v113", "v114", "v115", "v116", "v117", "v118", "v119", "v120", "v121", "v122", "v123", "v124", "v125", "v126", "v127", "v160", "s40", "s41", "s42", "s43", "s44", "s45", "s46", "s47", "s48", "s49", "s50", "s51", "s52", "s53", "scc", "memory"
#define K5_OUT(a, x) "=&{v[176:191]}"((a)[0]), "=&{v[192:207]}"((a)[1]), "=&{v[208:223]}"((a)[2]), "=&{v[224:239]}"((a)[3]), "=&{v[128:143]}"((x)[0]), "=&{v[144:159]}"((x)[1])
#define K5_LDS 135168
#define K5_LAST 67584
#define K5_SS 33792
#define K5_NBAR 13


typedef unsigned short u16;
typedef float f32x16 __attribute__((ext_vector_type(16)));
typedef float f32x4 __attribute__((ext_vector_type(4)));
typedef float f32x2 __attribute__((ext_vector_type(2)));
typedef __bf16 bf16x8 __attribute__((ext_vector_type(8)));
typedef __bf16 bf16x2 __attribute__((ext_vector_type(2)));
typedef short s16x4 __attribute__((ext_vector_type(4)));
typedef unsigned u32x4 __attribute__((ext_vector_type(4)));
typedef unsigned u32x2 __attribute__((ext_vector_type(2)));
#define DI __device__ __forceinline__
#define LAS __attribute__((address_space(3)))

constexpr int S = 2048, D = 1024, F = 3584;
constexpr float L2E = 1.4426950408889634f;

DI float lo2f(unsigned w) { return __uint_as_float(w << 16); }
DI float hi2f(unsigned w) { return __uint_as_float(w & 0xffff0000u); }
DI f32x2 up2(unsigned w) { return (f32x2){lo2f(w), hi2f(w)}; }
DI unsigned pkv(f32x2 v) { return __builtin_bit_cast(unsigned, __builtin_convertvector(v, bf16x2)); }
DI unsigned pk2(float a, float b) { return pkv((f32x2){a, b}); }
DI f32x2 rb2(f32x2 v) { return up2(pkv(v)); }
DI float rb(float f) { return lo2f(pk2(f, f)); }
DI f32x2 bc2(float a) { return (f32x2){a, a}; }
DI f32x2 fma2(f32x2 a, f32x2 b, f32x2 c) { return __builtin_elementwise_fma(a, b, c); }
DI unsigned rfl(unsigned x) { return __builtin_amdgcn_readfirstlane(x); }
DI const void* rflp(const void* p) { const unsigned long long v = (unsigned long long)p; return (const void*)(((unsigned long long)rfl((unsigned)(v >> 32)) << 32) | rfl((unsigned)v)); }
DI float ex2(float x) { return __builtin_amdgcn_exp2f(x); }
DI float rcp(float x) { return __builtin_amdgcn_rcpf(x); }
DI float dot2(unsigned a, unsigned b, float c) { return __builtin_amdgcn_fdot2_f32_bf16(__builtin_bit_cast(bf16x2, a), __builtin_bit_cast(bf16x2, b), c, false); }
DI float add32(float v) { auto r = __builtin_amdgcn_permlane32_swap(__float_as_uint(v), __float_as_uint(v), false, false); return __uint_as_float(r[0]) + __uint_as_float(r[1]); }
DI float max32(float v) { auto r = __builtin_amdgcn_permlane32_swap(__float_as_uint(v), __float_as_uint(v), false, false); return fmaxf(__uint_as_float(r[0]), __uint_as_float(r[1])); }
DI float add16(float v) { auto r = __builtin_amdgcn_permlane16_swap(__float_as_uint(v), __float_as_uint(v), false, false); return __uint_as_float(r[0]) + __uint_as_float(r[1]); }
template <int CTRL> DI float dpp(float v) { return __uint_as_float(__builtin_amdgcn_update_dpp(0u, __float_as_uint(v), CTRL, 0xf, 0xf, true)); }
DI float wave_sum(float v) {
  v += dpp<0xb1>(v); v += dpp<0x4e>(v); v += dpp<0x124>(v); v += dpp<0x128>(v);
  return add32(add16(v));
}
#define MFMA32(a, b, c) __builtin_amdgcn_mfma_f32_32x32x16_bf16(__builtin_bit_cast(bf16x8, a), __builtin_bit_cast(bf16x8, b), c, 0, 0, 0)
#define MFMA16(a, b, c) __builtin_amdgcn_mfma_f32_16x16x32_bf16(__builtin_bit_cast(bf16x8, a), __builtin_bit_cast(bf16x8, b), c, 0, 0, 0)
DI u32x2 trread(const LAS char* p) { s16x4 r = __builtin_amdgcn_ds_read_tr16_b64_v4i16((LAS s16x4*)p); return __builtin_bit_cast(u32x2, r); }
typedef __amdgpu_buffer_rsrc_t rsrc_t;
DI rsrc_t mkrsrc(const void* p, unsigned bytes) { return __builtin_amdgcn_make_buffer_rsrc((void*)p, 0, bytes, 0x20000); }
DI void st16_dev(rsrc_t r, unsigned off, u32x4 v) { __builtin_amdgcn_raw_buffer_store_b128(v, r, off, 0, 16); }
DI u32x4 ld16_dev(rsrc_t r, unsigned off) { return __builtin_amdgcn_raw_buffer_load_b128(r, off, 0, 16); }
DI void st4_dev(rsrc_t r, unsigned off, unsigned v) { __builtin_amdgcn_raw_buffer_store_b32(v, r, off, 0, 16); }
constexpr unsigned OOB = 0x80000000u;
DI unsigned ld4_dev(rsrc_t r, unsigned off) { return __builtin_amdgcn_raw_buffer_load_b32(r, off, 0, 16); }
DI void wait_ge(unsigned* flag, unsigned want) {
  _Pragma("nounroll") for (unsigned spin = 0; spin < (1u << 16); ++spin) {
    if (rfl(__hip_atomic_load(flag, __ATOMIC_RELAXED, __HIP_MEMORY_SCOPE_AGENT)) >= want) break;
    __builtin_amdgcn_s_sleep(1);
  }
}
constexpr unsigned FILL = 0xffffffffu;
template <int N, class OFF> DI void ld_again(u32x4 (&v)[N], rsrc_t r, OFF off) {
  _Pragma("nounroll") for (unsigned spin = 0; spin < (1u << 16); ++spin) {
    bool miss[N], any = false; u32x4 t[N];
#pragma unroll
    for (int i = 0; i < N; ++i) { miss[i] = __any(max(max(v[i][0], v[i][1]), max(v[i][2], v[i][3])) == FILL); any |= miss[i]; }
    if (!any) break;
    __builtin_amdgcn_s_sleep(3);
    asm volatile("" ::: "memory");
#pragma unroll
    for (int i = 0; i < N; ++i) t[i] = ld16_dev(r, miss[i] ? off(i) : OOB);
#pragma unroll
    for (int i = 0; i < N; ++i) v[i] = miss[i] ? t[i] : v[i];
  }
}
#define BAR() do { asm volatile("s_waitcnt lgkmcnt(0)" ::: "memory"); __builtin_amdgcn_s_barrier(); asm volatile("" ::: "memory"); } while (0)
#define PAIR(v, i) ((f32x2){(v)[i], (v)[(i) + 1]})
#define FENCE() __builtin_amdgcn_sched_barrier(0)

constexpr size_t MB = 1 << 20;
constexpr size_t WS_MODP  = 0;
constexpr size_t WS_MODR  = 1 * MB;
constexpr size_t WS_ST2   = 256 << 10;
constexpr size_t WS_FLAG  = 768 << 10;
constexpr size_t WS_GO    = 832 << 10;
constexpr size_t WS_H1    = 2 * MB;
constexpr size_t WS_WQ    = 7 * MB;
constexpr size_t WS_XCH   = 14 * MB;
constexpr size_t WS_KVP   = 22 * MB;
constexpr size_t WS_O     = 25 * MB;
constexpr size_t WS_A2    = 35 * MB;
constexpr size_t WS_HH    = 40 * MB;
constexpr unsigned KVB = 2112 * 4;
enum { C_H = 0, C_O = 8, C_A = 16, C_U = 24, C_M = 40, C_W = 44 };
#define RLX __ATOMIC_RELAXED, __HIP_MEMORY_SCOPE_AGENT
DI unsigned* flagp(char* ws, int i, int r) { return (unsigned*)(ws + WS_FLAG + (i * 8 + r) * 128); }
DI void count(char* ws, int i) {
  if (rfl(threadIdx.x >> 6) == 0) asm volatile("s_nop 4\n buffer_atomic_add %0, %1, %2, 0 offen" :: "v"(1u), "v"(threadIdx.x * 128), "s"(mkrsrc(ws + WS_FLAG + i * 1024, 1024)) : "memory"); }
DI void arrive(char* ws, int i) { asm volatile("s_waitcnt vmcnt(0)" ::: "memory"); BAR(); count(ws, i); }
DI void await(char* ws, int i, unsigned n) { if (rfl(threadIdx.x >> 6) == 0) wait_ge(flagp(ws, i, blockIdx.x & 7), n); asm volatile("s_waitcnt vmcnt(0)" ::: "memory"); BAR(); }
DI void wait_shards(char* ws, int i, int n, int lane) {
  _Pragma("nounroll") for (unsigned spin = 0; spin < (1u << 16); ++spin) {
    if (__all(__hip_atomic_load(flagp(ws, i + (lane & (n - 1)), blockIdx.x & 7), RLX) >= 32u)) break;
    __builtin_amdgcn_s_sleep(1); }
}

DI void ln_round(float mean, float var, float& m, float& sd) { m = rb(mean); sd = rb(__builtin_amdgcn_sqrtf(rb(rb(var) + 1e-5f))); }
DI unsigned silu2(unsigned w) { const f32x2 x = up2(w); return pk2(x[0] * rcp(1.f + ex2(-L2E * x[0])), x[1] * rcp(1.f + ex2(-L2E * x[1]))); }

template <class MID> DI void featmap(const f32x16& qt, const u32x4& w0, const u32x4& w1, u32x4 (&ph)[4], MID mid) {
  f32x16 p = {};
#pragma unroll
  for (int dt = 0; dt < 2; ++dt) {
    auto r0 = __builtin_amdgcn_permlane16_swap(pkv(PAIR(qt, 8 * dt)), pkv(PAIR(qt, 8 * dt + 4)), false, false), r1 = __builtin_amdgcn_permlane16_swap(pkv(PAIR(qt, 8 * dt + 2)), pkv(PAIR(qt, 8 * dt + 6)), false, false);
    p = MFMA32(dt ? w1 : w0, ((u32x4){r0[0], r1[0], r0[1], r1[1]}), p); }
  f32x2 x[8];
#pragma unroll
  for (int r = 0; r < 8; ++r) x[r] = rb2(PAIR(p, 2 * r));
  float mx = fmaxf(x[0][0], x[0][1]), mn = fminf(x[0][0], x[0][1]);
#pragma unroll
  for (int r = 1; r < 8; ++r) { mx = fmaxf(fmaxf(mx, x[r][0]), x[r][1]); mn = fminf(fminf(mn, x[r][0]), x[r][1]); }
  mx = max32(mx); mn = -max32(-mn);
  const f32x2 cp = bc2(-mx * L2E), cn = bc2(mn * L2E);
  f32x2 ep[8], en[8], sp = {0.f, 0.f}, sn = {0.f, 0.f};
#pragma unroll
  for (int r = 0; r < 8; ++r) { const f32x2 a = fma2(x[r], bc2(L2E), cp), b = fma2(x[r], bc2(-L2E), cn);
    ep[r] = (f32x2){ex2(a[0]), ex2(a[1])}; en[r] = (f32x2){ex2(b[0]), ex2(b[1])}; sp += ep[r]; sn += en[r]; }
  mid();
  const float rp = rcp(add32(sp[0] + sp[1])), rn = rcp(add32(sn[0] + sn[1]));
#pragma unroll
  for (int r = 0; r < 8; ++r) { ph[r >> 2][r & 3] = pkv(ep[r] * rp); ph[2 + (r >> 2)][r & 3] = pkv(en[r] * rn); }
}

constexpr int CODEL = 22;
constexpr int PFL = 21;
DI void dma16(const void* g, unsigned l) { asm volatile("s_mov_b32 m0, %1\n s_nop 0\n global_load_lds_dwordx4 %0, off" :: "v"(g), "s"(l) : "memory"); }
DI void touch(const void* g, unsigned l) { asm volatile("s_mov_b32 m0, %1\n s_nop 0\n global_load_lds_dword %0, off" :: "v"(g), "s"(l) : "memory"); }
DI void mod_final(const u32x4 (&p)[4], float one, float* dst, int lane) {
  f32x4 s = __builtin_bit_cast(f32x4, p[0]);
#pragma unroll
  for (int i = 1; i < 4; ++i) s += __builtin_bit_cast(f32x4, p[i]);
  const f32x2 a = rb2(rb2((f32x2){add32(add16(s[0])), add32(add16(s[1]))}) + one), b = rb2(rb2((f32x2){add32(add16(s[2])), add32(add16(s[3]))}) + one);
  if (lane < 16) *(f32x4*)(dst + lane * 4) = (f32x4){a[0], a[1], b[0], b[1]};
}

__global__ __launch_bounds__(512, 2) void k_main(const u16* __restrict__ x, const u16* __restrict__ wqkv, const u16* __restrict__ wphi, const u16* __restrict__ wo,
                                                 const u16* __restrict__ w1, const u16* __restrict__ w3, const u16* __restrict__ w2, const u16* __restrict__ c,
                                                 const u16* __restrict__ wmod, char* __restrict__ ws, u16* __restrict__ out) {
  __shared__ __attribute__((aligned(16))) char smem[163840];
  const char* code; asm volatile("s_getpc_b64 %0" : "=s"(code));
  const int wv = rfl(threadIdx.x >> 6), wm = wv & 3, wk = wv >> 2;
#define LANES() int tid = threadIdx.x; asm volatile("" : "+v"(tid)); const int lane = tid & 63, h = lane >> 5, g = lane >> 4; (void)h; (void)g
  const int xcd = blockIdx.x & 7, idx = blockIdx.x >> 3;
  LAS char* sm = (LAS char*)smem;
  const unsigned lbase = (unsigned)(size_t)sm;
  const rsrc_t wr = mkrsrc(ws, 64 * MB);
  const int head = (xcd >> 1) * 8 + (idx & 7), rb1 = (xcd & 1) * 4 + (idx >> 3);
  const int rbk2 = (xcd >> 1) * 4 + (idx & 3), kh = xcd & 1, cb5 = idx >> 2, cb2 = 2 * cb5 + kh;
  const int rg2 = rbk2 * 4 + wm;
  const int cb4 = xcd * 4 + (idx & 3), rb4 = idx >> 2, odd = cb4 & 1;
  constexpr int XT = 145408, TB = XT + 16384, PH = 157696;
  u32x4 x1[2];
  const char* bp1 = ws + WS_WQ + (size_t)head * 196608; unsigned m01 = lbase + wv * 1024;
  asm volatile("" : "+s"(bp1), "+s"(m01));

#define PRE1(i) { const unsigned g0 = wv * 1024 + lane * 16; \
    asm volatile(K1_PRE##i :: "s"(bp1), "s"(196608), "s"(m01), "s"(0), "v"(g0), "v"(g0 + 8192), "v"(g0 + 16384) : K1_PRECLOB); }
#define PRE2(i) { const unsigned g0 = (wv + 8 * (lane >> 3)) * 2048 + cb2 * 128 + (lane & 7) * 16; \
    asm volatile(K2_PRE##i :: "s"(wo), "s"(1024 * 2048), "s"(lbase + wv * 1056), "s"(0), "v"(g0), "v"(g0 + 64 * 2048) : K2_PRECLOB); }
#define G4() unsigned gg[4]; _Pragma("unroll") for (int i = 0; i < 4; ++i) { const unsigned L = 1024 * (wm + 4 * i) + 16 * lane, r = L / 224, rem = L % 224 + 32 * odd; \
    gg[i] = r * 7168 + cb4 * 224 + (rem >= 224 ? rem - 224 : rem); } const void* bp = rflp(wk ? w3 : w1)
#define G5() const unsigned g0 = (kh * 1792 + wv + 8 * g) * 2048 + cb5 * 256 + (((lane & 15) * 16) ^ (kh * 128))

  {
    LANES();
    const int b = blockIdx.x, ri = xcd * 16 + (idx >> 1);
    const unsigned gop = WS_GO + b * 4;
    if (idx & 1) {
      char* lm = smem + K1_LDS;
      st4_dev(wr, wv != 0 ? OOB : lane == 0 ? gop : lane < 4 && ri * 3 + lane <= 320 ? WS_FLAG + (ri * 3 + lane - 1) * 128 : OOB, 0u);
      asm volatile("" ::: "memory");
      const int kq = ri & 3, mcol = (ri >> 2) * 64, krow = kq * 256 + (tid >> 3) * 4;
      const u16* wp = wmod + (size_t)krow * 6144 + mcol + (tid & 7) * 8;
      u32x4 wa[4], v[2][2], wt[2];
#pragma unroll
      for (int i = 0; i < 4; ++i) wa[i] = *(const u32x4*)(wp + i * 6144);
      const u32x2 cv = *(const u32x2*)(c + krow);
      asm volatile("" ::: "memory");
      __builtin_amdgcn_s_barrier();
      asm volatile("" ::: "memory");
      const int rowb = rb1 * 16 + (head >> 1);
      const u16* xr = x + (size_t)(rowb * 16 + wv * 2) * D + lane * 8;
#pragma unroll
      for (int i = 0; i < 4; ++i) v[i >> 1][i & 1] = *(const u32x4*)(xr + i * 512);
      { const rsrc_t qr = mkrsrc(wphi, wv == 1 ? 65536 : 0);
#pragma unroll
        for (int i = 0; i < 2; ++i) wt[i] = __builtin_amdgcn_raw_buffer_load_b128(qr, head * 2048 + i * 1024 + lane * 16, 0, 0); }
      asm volatile("" ::: "memory"); FENCE();
      float* red = (float*)(smem + TB);
      { float a[8] = {};
#pragma unroll
        for (int i = 0; i < 2; ++i) { const unsigned s2 = silu2(cv[i]), l0 = s2 & 0xffffu, h0 = s2 << 16, l1 = s2 >> 16, h1 = s2 & 0xffff0000u;
#pragma unroll
          for (int e = 0; e < 4; ++e) { a[2 * e] = dot2(wa[2 * i + 1][e], l1, dot2(wa[2 * i][e], l0, a[2 * e])); a[2 * e + 1] = dot2(wa[2 * i + 1][e], h1, dot2(wa[2 * i][e], h0, a[2 * e + 1])); } }
#pragma unroll
        for (int e = 0; e < 4; ++e) { auto r = __builtin_amdgcn_permlane32_swap(__float_as_uint(a[e]), __float_as_uint(a[e + 4]), false, false); a[e] = __uint_as_float(r[0]) + __uint_as_float(r[1]); }
#pragma unroll
        for (int e = 0; e < 2; ++e) { auto r = __builtin_amdgcn_permlane16_swap(__float_as_uint(a[e]), __float_as_uint(a[e + 2]), false, false); a[e] = __uint_as_float(r[0]) + __uint_as_float(r[1]);
          a[e] += dpp<0x128>(a[e]); }
        if ((lane & 8) == 0) *(f32x2*)(red + wv * 64 + (lane & 7) * 8 + 4 * h + 2 * (g & 1)) = (f32x2){a[0], a[1]}; }
      asm volatile("s_waitcnt vmcnt(6)" ::: "memory");
      unsigned go = ld4_dev(wr, wv == 0 ? gop : OOB);
      BAR();
      if (wv == 7) go = ld4_dev(wr, gop);
      if (wv == 0) { float s = 0;
#pragma unroll
        for (int i = 0; i < 8; ++i) s += red[i * 64 + lane];
        __hip_atomic_store((float*)(ws + WS_MODP) + kq * 2048 + mcol + lane, s, RLX);
        FENCE();
        if (rfl(go) == 0) wait_ge((unsigned*)(ws + gop), 1);
        asm volatile("s_waitcnt vmcnt(0)" ::: "memory");
        count(ws, C_M + kq);
        __builtin_amdgcn_s_setprio(2); }
      f32x2 f[2][8];
#pragma unroll
      for (int r = 0; r < 2; ++r) {
#pragma unroll
        for (int e = 0; e < 8; ++e) f[r][e] = up2(v[r][e >> 2][e & 3]);
        const f32x2 s2 = ((f[r][0] + f[r][1]) + (f[r][2] + f[r][3])) + ((f[r][4] + f[r][5]) + (f[r][6] + f[r][7]));
        const float mean = wave_sum(s2[0] + s2[1]) * (1.f / D);
        f32x2 q2 = {0.f, 0.f};
#pragma unroll
        for (int e = 0; e < 8; ++e) { const f32x2 d = f[r][e] - mean; q2 = fma2(d, d, q2); }
        float m, sd; ln_round(mean, wave_sum(q2[0] + q2[1]) * (1.f / D), m, sd);
        const float rs = rcp(sd);
#pragma unroll
        for (int hf = 0; hf < 2; ++hf) { u32x4 o;
#pragma unroll
          for (int e = 0; e < 4; ++e) o[e] = pkv(rb2(f[r][4 * hf + e] - m) * rs);
          *(u32x4*)(lm + 8192 + (hf * 64 + lane) * 272 + (wv * 2 + r) * 16) = o; } }
      if (wv == 1) { *(u32x4*)(smem + PH + lane * 16) = wt[0]; *(u32x4*)(smem + PH + 1024 + lane * 16) = wt[1]; }
      __builtin_amdgcn_s_setprio(0);
      if (wv == 7) { if (rfl(go) == 0) wait_ge((unsigned*)(ws + gop), 1); wait_shards(ws, C_M, 4, lane); }
      BAR();
      u32x4 mq[4];
#pragma unroll
      for (int i = 0; i < 4; ++i) mq[i] = ld16_dev(wr, WS_MODP + i * 8192 + h * 4096 + wv * 512 + (lane & 31) * 16);
      FENCE();
      f32x2 xh[4][4];
#pragma unroll
      for (int i = 0; i < 4; ++i) { const u32x4 t = *(const u32x4*)(lm + 8192 + (wv * 16 + i * 4 + g) * 272 + (lane & 15) * 16);
#pragma unroll
        for (int e = 0; e < 4; ++e) { xh[i][e] = up2(t[e]); asm volatile("" : "+v"(xh[i][e])); } }
      FENCE();
      {
        const f32x4 mv = (__builtin_bit_cast(f32x4, mq[0]) + __builtin_bit_cast(f32x4, mq[1])) + (__builtin_bit_cast(f32x4, mq[2]) + __builtin_bit_cast(f32x4, mq[3]));
        const float one = h ? 1.f : 0.f;
        const f32x2 a = rb2(rb2(PAIR(mv, 0)) + one), c2 = rb2(rb2(PAIR(mv, 2)) + one);
        *(f32x4*)(lm + tid * 16) = (f32x4){a[0], a[1], c2[0], c2[1]}; }
      asm volatile("s_waitcnt lgkmcnt(0)" ::: "memory");
#pragma unroll
      for (int i = 0; i < 4; ++i) { const float* sp = (const float*)(lm + wv * 1024) + (i * 4 + g) * 8;
        const f32x4 h0 = *(const f32x4*)sp, h1 = *(const f32x4*)(sp + 4), c0 = *(const f32x4*)(sp + 128), c1 = *(const f32x4*)(sp + 132);
        u32x4 o;
        o[0] = pkv(rb2(xh[i][0] * PAIR(c0, 0)) + PAIR(h0, 0)); o[1] = pkv(rb2(xh[i][1] * PAIR(c0, 2)) + PAIR(h0, 2));
        o[2] = pkv(rb2(xh[i][2] * PAIR(c1, 0)) + PAIR(h1, 0)); o[3] = pkv(rb2(xh[i][3] * PAIR(c1, 2)) + PAIR(h1, 2));
        st16_dev(wr, WS_H1 + (rowb >> 1) * 65536 + (rowb & 1) * 1024 + (wv * 4 + i) * 2048 + lane * 16, o); }
      arrive(ws, C_H + rb1);
    } else {
      const bool srv = b == 0 && wv == 1;
      constexpr int WI = K1_LDS;
      st4_dev(wr, tid == 0 ? gop : OOB, 0u);
      if (srv) { st4_dev(wr, WS_FLAG + (C_M * 8 + lane) * 128, 0u); asm volatile("s_waitcnt vmcnt(0)" ::: "memory"); }
      const u32x4 one = {1u, 1u, 1u, 1u};
#define GO_ALL() if (srv) st16_dev(wr, WS_GO + lane * 16, one)
      GO_ALL();
      if (wv == 2) touch(code + ((ri & 15) * CODEL + (lane < CODEL ? lane : 0)) * 128, lbase + TB);
      { const u32x4 fill = {FILL, FILL, FILL, FILL};
        const unsigned ko = WS_KVP + ri * 2 * KVB;
        st16_dev(wr, ko + tid * 16, fill); st16_dev(wr, ko + 8192 + tid * 16, fill);
        st16_dev(wr, tid < 32 ? ko + 16384 + tid * 16 : tid >= 64 && tid < 192 ? WS_ST2 + ri * 2048 + (tid - 64) * 16 : OOB, fill); }
      const int mb = xcd * 8 + wv, mk = ri & 15;
      u32x4 sf[2] = {*(const u32x4*)(c + mk * 64 + 8 * g), *(const u32x4*)(c + mk * 64 + 32 + 8 * g)};
      GO_ALL();
      const int kb = ri >> 1, cf = ri & 1;
#pragma unroll
      for (int i = 0; i < 6; ++i) { const int r = wv * 2 + i / 3;
        dma16(wqkv + (size_t)(kb * 16 + r) * 3072 + cf * 1536 + (i % 3) * 512 + lane * 8, lbase + WI + r * 3136 + (i % 3) * 1024); }
      if (wv < 2) dma16(wphi + head * 1024 + wv * 512 + lane * 8, lbase + PH + wv * 1024);
      if (srv) { __builtin_amdgcn_s_sleep(10); GO_ALL(); __builtin_amdgcn_s_sleep(10); GO_ALL(); }
      asm volatile("s_waitcnt vmcnt(0)" ::: "memory");
      BAR();
      GO_ALL();
      const unsigned go = ld4_dev(wr, wv == 0 ? gop : OOB);
      { const LAS char* tp = sm + WI + (8 * h + ((lane & 15) >> 2)) * 3136 + wv * 384 + (g & 1) * 32 + (lane & 3) * 8;
#pragma unroll
        for (int i = 0; i < 6; ++i) { const int t32 = cf * 48 + wv * 6 + i; const u32x2 a = trread(tp + i * 64), bb = trread(tp + i * 64 + 4 * 3136);
          st16_dev(wr, WS_WQ + (((t32 & 31) * 32 + (kb >> 1)) * 6 + (t32 >> 5) * 2 + (g & 1)) * 1024 + ((kb & 1) * 2 + h) * 256 + (lane & 15) * 16, (u32x4){a[0], a[1], bb[0], bb[1]}); } }
      if (wv == 0 && b != 0 && rfl(go) == 0) wait_ge((unsigned*)(ws + gop), 1);
      asm volatile("" : "+v"(sf[0]), "+v"(sf[1]) :: "memory");
      arrive(ws, C_W + (ri & 3));
#pragma unroll
      for (int i = 0; i < 8; ++i) dma16(wmod + (size_t)(mk * 64 + i * 8 + (lane >> 3)) * 6144 + 2048 + mb * 64 + (lane & 7) * 8, lbase + wv * 8192 + i * 1024);
      { const LAS char* pb = sm + wv * 8192 + (8 * g + ((lane & 15) >> 2)) * 128 + (lane & 3) * 8;
#pragma unroll
        for (int i = 0; i < 8; ++i) sf[i >> 2][i & 3] = silu2(sf[i >> 2][i & 3]);
        asm volatile("s_waitcnt vmcnt(0)" ::: "memory");
        f32x4 r[4] = {};
#pragma unroll
        for (int ks = 0; ks < 2; ++ks)
#pragma unroll
          for (int nt = 0; nt < 4; ++nt) { const u32x2 b0 = trread(pb + ks * 4096 + nt * 32), b1 = trread(pb + ks * 4096 + nt * 32 + 512);
            r[nt] = MFMA16(sf[ks], ((u32x4){b0[0], b0[1], b1[0], b1[1]}), r[nt]); }
        const float lo = (g & 1) ? r[1][0] : r[0][0], hi = (g & 1) ? r[3][0] : r[2][0];
        st4_dev(wr, WS_MODR + (mb * 16 + mk) * 256 + lane * 4, __float_as_uint(h ? hi : lo)); }
      if (srv) _Pragma("nounroll") for (unsigned spin = 0; spin < (1u << 16); ++spin) {
        GO_ALL();
        if (__all(__hip_atomic_load(flagp(ws, C_M + (lane & 7), 0), RLX) >= 32u)) break;
        __builtin_amdgcn_s_sleep(2); }
      if (wv == 0) wait_shards(ws, C_M, 8, lane);
      BAR();
      PRE1(0)
    }
  }
  {
    LANES();
    constexpr int PS = 136, PO = K1_LDS, VO = PO + 256 * PS, KO = VO + 16384, ZO = KO + 4096;
    const int q = (lane & 15) >> 2, p = lane & 3;
    f32x16 acc[3];
    u32x4 wf[2];
#pragma unroll
    for (int s = 0; s < 2; ++s) { const LAS char* tp = sm + PH + (16 * s + 8 * h + q) * 64 + (g & 1) * 32 + p * 8; const u32x2 a = trread(tp), bb = trread(tp + 256); wf[s] = (u32x4){a[0], a[1], bb[0], bb[1]}; }
    asm volatile("" : "+v"(wf[0]), "+v"(wf[1]));
    { unsigned va = lane * 16, vb = lbase + va, g0 = wv * 1024 + lane * 16, g1 = g0 + 8192, g2 = g0 + 16384;
      unsigned n0 = (wv + 8 * (lane >> 3)) * 2048 + cb2 * 128 + (lane & 7) * 16;
      const rsrc_t r2 = mkrsrc(wo, 1024 * 2048);
      const int l5 = (lane & 31) < PFL ? (lane & 31) : 0, tw = idx * 8 + wv;
      const char* p2 = (const char*)(lane < 32 ? w1 : w3) + (size_t)((l5 / 7) * 256 + tw) * 7168 + (xcd * 7 + l5 % 7) * 128;
      if ((lane & 31) >= 28) p2 = (const char*)wo + (size_t)((lane & 3) * 256 + tw) * 2048 + ((xcd >> 1) * 4 + h * 2 + kh) * 128;
      const char* ap = ws + WS_H1 + (size_t)(rb1 * 8 + wv) * 65536;
      asm volatile("" : "+v"(va), "+v"(vb), "+v"(g0), "+v"(g1), "+v"(g2), "+v"(n0), "+v"(p2), "+s"(ap));
      if (wv == 0) wait_ge(flagp(ws, C_H + rb1, xcd), 16);
      if (wv == 1 && (idx & 1)) wait_shards(ws, C_W, 4, lane);
      BAR();
      if (idx & 1) PRE1(0)
      asm volatile(K1_ASM : K1_OUT(acc, acc)
                   : "s"(ap), "s"(65536), "s"(bp1), "s"(196608), "s"(m01), "s"(0), "v"(va), "v"(vb), "v"(g0), "v"(g1), "v"(g2),
                     "v"(n0), "s"(r2), "s"(lbase + wv * 1056), "v"(p2), "s"(rfl(lbase + TB + wv * 256)) : K1_CLOB); }
    u32x4 ph[4];
#pragma unroll
    for (int dt = 0; dt < 2; ++dt)
      *(u32x4*)(smem + VO + (dt * 8 + wv) * 1024 + lane * 16) = (u32x4){pkv(PAIR(acc[2], 8 * dt)), pkv(PAIR(acc[2], 8 * dt + 2)), pkv(PAIR(acc[2], 8 * dt + 4)), pkv(PAIR(acc[2], 8 * dt + 6))};
    featmap(acc[1], wf[0], wf[1], ph, [] {});
    { char* rowp = smem + PO + (wv * 32 + (lane & 31)) * PS + 8 * h;
#pragma unroll
      for (int fb = 0; fb < 4; ++fb) { *(u32x2*)(rowp + fb * 32) = (u32x2){ph[fb][0], ph[fb][1]}; *(u32x2*)(rowp + fb * 32 + 16) = (u32x2){ph[fb][2], ph[fb][3]}; } }
    BAR();
    const unsigned kvo = WS_KVP + head * 8 * KVB;
    { const int ft = wm, dt = wk;
      const u32x4 ones = {0x3f803f80u, 0x3f803f80u, 0x3f803f80u, 0x3f803f80u};
      const LAS char* pa = sm + PO + (lane >> 2) * PS + ft * 32 + p * 8;
      const char* vb = smem + VO + dt * 8192 + lane * 16;
      u32x4 a[8], bv[8];
#pragma unroll
      for (int ks = 0; ks < 8; ++ks) { const u32x2 a0 = trread(pa + ks * 32 * PS), a1 = trread(pa + (ks * 32 + 16) * PS);
        a[ks] = (u32x4){a0[0], a0[1], a1[0], a1[1]}; bv[ks] = *(const u32x4*)(vb + ks * 1024); }
      FENCE();
      f32x4 kv = {}, z = {};
#pragma unroll
      for (int ks = 0; ks < 8; ++ks) kv = MFMA16(a[ks], bv[ks], kv);
      if (dt == 0) {
#pragma unroll
        for (int ks = 0; ks < 8; ++ks) z = MFMA16(a[ks], ones, z); }
      st16_dev(wr, kvo + rb1 * KVB + ((ft * 2 + (g >> 1)) * 64 + dt * 16 + (lane & 15) + 32 * (g & 1)) * 16, __builtin_bit_cast(u32x4, kv));
      if (dt == 0 && (lane & 15) == 0) st16_dev(wr, kvo + rb1 * KVB + 8192 + ft * 64 + g * 16, __builtin_bit_cast(u32x4, z)); }
    u32x4 pv[9];
    const unsigned o0 = kvo + tid * 16, oz = kvo + (lane >> 3) * KVB + 8192 + (wv & 1) * 128 + (lane & 7) * 16;
    featmap(acc[0], wf[0], wf[1], ph, [&] { FENCE();
#pragma unroll
      for (int i = 0; i < 9; ++i) pv[i] = ld16_dev(wr, i < 8 ? o0 + i * KVB : oz);
      FENCE(); });
    asm volatile("" : "+v"(ph[0]), "+v"(ph[1]), "+v"(ph[2]), "+v"(ph[3]));
    { FENCE();
      ld_again<9>(pv, wr, [&](int i) { return i < 8 ? o0 + i * KVB : oz; });
      f32x4 s = __builtin_bit_cast(f32x4, pv[0]);
#pragma unroll
      for (int i = 1; i < 8; ++i) s += __builtin_bit_cast(f32x4, pv[i]);
      *(u32x2*)(smem + KO + ((wv >> 1) * 64 + lane) * 16 + (wv & 1) * 8) = (u32x2){pk2(s[0], s[1]), pk2(s[2], s[3])};
      if (wv < 2) { f32x4 z = __builtin_bit_cast(f32x4, pv[8]);
#pragma unroll
        for (int e = 0; e < 4; ++e) z[e] = add32(add16(z[e] + dpp<0x128>(z[e])));
        if (lane < 8) *(u32x2*)(smem + ZO + wv * 64 + lane * 8) = (u32x2){pk2(z[0], z[1]), pk2(z[2], z[3])}; } }
    PRE2(1)
    BAR();
    { u32x4 a[4], za[4];
#pragma unroll
      for (int fb = 0; fb < 4; ++fb) { const u32x2 z0 = *(const u32x2*)(smem + ZO + fb * 32 + 8 * h), z1 = *(const u32x2*)(smem + ZO + fb * 32 + 16 + 8 * h);
        za[fb] = (u32x4){z0[0], z0[1], z1[0], z1[1]}; a[fb] = *(const u32x4*)(smem + KO + (fb * 64 + lane) * 16); }
      FENCE();
      f32x16 num = {}, den = {};
#pragma unroll
      for (int fb = 0; fb < 4; ++fb) den = MFMA32(za[fb], ph[fb], den);
#pragma unroll
      for (int fb = 0; fb < 4; ++fb) num = MFMA32(a[fb], ph[fb], num);
      const float rd = rcp(rb(rb(den[0]) + 1e-6f));
      u32x4 o[2];
#pragma unroll
      for (int r = 0; r < 8; ++r) o[(r >> 1) & 1][2 * (r >> 2) + (r & 1)] = pkv(rb2(PAIR(num, 2 * r)) * rd);
      const unsigned oo = WS_O + (rb1 * 8 + wv) * 65536 + head * 2048 + (g & 1) * 1024 + ((lane & 15) + 16 * h) * 16;
      st16_dev(wr, oo, o[0]); st16_dev(wr, oo + 512, o[1]); }
    arrive(ws, C_O + rb1);
    PRE2(2)
  }
  {
    LANES();
    f32x16 acc[1];
    float* gl = (float*)(smem + TB);
    { unsigned va = lane * 16, vb = lbase + ((lane >> 2) & 7) * 1056 + h * 128 + 8 * (lane & 3);
      const int xrow = (wm * 2 + wk) * 16 + (lane >> 3), sw = (lane & 7) ^ ((xrow >> 1) & 7);
      const u16* xs = x + (size_t)(rbk2 * 128 + xrow) * D + cb2 * 64;
      const u16 *x0 = xs + sw * 8, *x8 = xs + 8 * D + (sw ^ 4) * 8;
      const char* ap = ws + WS_O + (size_t)rg2 * 65536 + wk * 1024;
      G4();
      const rsrc_t r4 = mkrsrc(bp, 1024 * 7168);
      asm volatile("" : "+v"(va), "+v"(vb), "+v"(x0), "+v"(x8), "+v"(gg[0]), "+v"(gg[1]), "+v"(gg[2]), "+v"(gg[3]), "+s"(ap));
      await(ws, C_O + (rbk2 >> 1), 32);
      if (wk == 0) { u32x4 mq[4];
#pragma unroll
        for (int i = 0; i < 4; ++i) mq[i] = ld16_dev(wr, WS_MODR + (wm * 16 + cb2) * 4096 + g * 1024 + i * 256 + (lane & 15) * 16);
        mod_final(mq, wm == 2 ? 1.f : 0.f, gl + wm * 64, lane); }
      asm volatile(K2_ASM : K2_OUT(acc, acc) : "s"(ap), "s"(65536 - 1024 * wk), "s"(wo), "s"(0), "s"(0), "s"(0), "v"(va), "v"(vb), "v"(va), "v"(va),
                   "v"(x0), "v"(x8), "s"(rfl(lbase + XT + wv * 2048)),
                   "v"(gg[0]), "v"(gg[1]), "v"(gg[2]), "v"(gg[3]), "s"(r4), "s"(rfl(lbase + wk * 14336 + wm * 1024)), "s"(rfl((wm >> 1) ^ 1)) : K2_CLOB); }
    const float* gp = gl + 4 * g;
#pragma unroll
    for (int t = 0; t < 4; ++t) { const f32x4 gv = *(const f32x4*)(gp + 16 * t);
      const u32x2 xr = *(const u32x2*)(smem + XT + wv * 2048 + (lane & 15) * 128 + ((2 * t + (g >> 1)) ^ ((lane >> 1) & 7)) * 16 + 8 * (g & 1));
#pragma unroll
      for (int e = 0; e < 2; ++e) x1[t >> 1][2 * (t & 1) + e] = pkv(up2(xr[e]) + rb2(PAIR(gv, 2 * e) * rb2(PAIR(acc[0], 4 * t + 2 * e)))); }
    const unsigned so = WS_ST2 + (rg2 * 2 + wk) * 2048;
    { const u32x4 ones = {0x3f803f80u, 0x3f803f80u, 0x3f803f80u, 0x3f803f80u}; const f32x4 z4 = {0.f, 0.f, 0.f, 0.f};
      const f32x4 s2 = MFMA16(x1[1], x1[1], MFMA16(x1[0], x1[0], z4)), s1 = MFMA16(ones, x1[1], MFMA16(ones, x1[0], z4));
      const float lo = (lane & 1) ? s2[1] : s2[0], hi = (lane & 1) ? s2[3] : s2[2];
      __builtin_amdgcn_raw_buffer_store_b64((u32x2){__float_as_uint(s1[0]), __float_as_uint((lane & 2) ? hi : lo)}, wr,
                                            g == ((lane & 15) >> 2) ? so + cb2 * 128 + (lane & 15) * 8 : OOB, 0, 16); }
    { const u32x4 fill = {FILL, FILL, FILL, FILL}; const unsigned xo = WS_XCH + blockIdx.x * 16384 + wv * 2048 + lane * 16;
      st16_dev(wr, xo, fill); st16_dev(wr, xo + 1024, fill); }
    __builtin_amdgcn_s_sleep(6);
    f32x4 sv[4], cv[4]; f32x2 xf[8];
#pragma unroll
    for (int t = 0; t < 4; ++t) { sv[t] = *(const f32x4*)(gp + 64 + 16 * t); cv[t] = *(const f32x4*)(gp + 128 + 16 * t); asm volatile("" : "+v"(sv[t]), "+v"(cv[t])); }
#pragma unroll
    for (int i = 0; i < 8; ++i) { xf[i] = up2(x1[i >> 2][i & 3]); asm volatile("" : "+v"(xf[i])); }
    u32x2 su[4];
    _Pragma("nounroll") for (unsigned spin = 0; spin < (1u << 16); ++spin) {
#pragma unroll
      for (int i = 0; i < 4; ++i) su[i] = __builtin_amdgcn_raw_buffer_load_b64(wr, so + i * 512 + lane * 8, 0, 16);
      if (!__any(max(max(max(su[0][0], su[0][1]), max(su[1][0], su[1][1])), max(max(su[2][0], su[2][1]), max(su[3][0], su[3][1]))) == FILL)) break;
      __builtin_amdgcn_s_sleep(3);
      asm volatile("" ::: "memory"); }
    { const f32x2 a = (__builtin_bit_cast(f32x2, su[0]) + __builtin_bit_cast(f32x2, su[1])) + (__builtin_bit_cast(f32x2, su[2]) + __builtin_bit_cast(f32x2, su[3]));
      const float mean = add32(add16(a[0])) * (1.f / D);
      float mm, sd; ln_round(mean, fmaxf(add32(add16(a[1])) * (1.f / D) - mean * mean, 0.f), mm, sd);
      const float rs = rcp(sd);
#pragma unroll
      for (int p = 0; p < 2; ++p) { u32x4 hv;
#pragma unroll
        for (int d = 0; d < 4; ++d) hv[d] = pkv(fma2((xf[4 * p + d] - mm) * rs, PAIR(cv[2 * p + (d >> 1)], 2 * (d & 1)), PAIR(sv[2 * p + (d >> 1)], 2 * (d & 1))));
        st16_dev(wr, WS_A2 + (rg2 * 32 + cb2 * 2 + p) * 2048 + wk * 1024 + lane * 16, hv); } }
    arrive(ws, C_A + (rbk2 >> 1));
  }
  {
    LANES();
    const int rg = rb4 * 8 + wv;
    f32x16 acc[7];
    { G4(); G5();
      unsigned va = lane * 16, vb = lbase + (lane >> 2) * 224 + 8 * (lane & 3), n0 = g0;
      const char* p0 = (const char*)w2 + (size_t)(kh * 1792 + (lane < 28 ? lane >> 2 : 0) * 256 + idx * 8 + wv) * 2048 + ((xcd >> 1) * 4 + (lane & 3)) * 128;
      const char* ap = ws + WS_A2 + (size_t)rg * 65536;
      const rsrc_t r5 = mkrsrc(w2, F * 2048);
      asm volatile("" : "+v"(va), "+v"(vb), "+v"(gg[0]), "+v"(gg[1]), "+v"(gg[2]), "+v"(gg[3]), "+v"(p0), "+v"(n0), "+s"(ap), "+s"(bp));
      await(ws, C_A + rb4, 32);
      asm volatile(K4_ASM : K4_OUT(acc, acc)
                   : "s"(ap), "s"(65536), "s"(bp), "s"(1024 * 7168), "s"(lbase + wk * 14336 + wm * 1024), "s"((wm >> 1) ^ 1),
                     "v"(va), "v"(vb), "v"(gg[0]), "v"(gg[1]), "v"(gg[2]), "v"(gg[3]),
                     "v"(p0), "s"(lbase + K4_LDS + wv * 256), "v"(n0), "s"(r5), "s"(lbase + wv * 1056) : K4_CLOB); }
    int t2 = threadIdx.x; asm volatile("" : "+v"(t2));
    const unsigned ho = WS_HH + rg * (F * 64) + (t2 & 63) * 16;
    unsigned yw[2][4];
#pragma unroll
    for (int c = 0; c < 7; ++c) {
#pragma unroll
      for (int m = 0; m < 2; ++m)
#pragma unroll
        for (int e = 0; e < 2; ++e) {
          const f32x2 a = PAIR(acc[c], 4 * m + 2 * e), b = PAIR(acc[c], 8 + 4 * m + 2 * e);
          const f32x2 z = a * (-L2E), d = (f32x2){ex2(z[0]), ex2(z[1])} + 1.f;
          yw[m][2 * (c & 1) + e] = pkv((a * b) * (f32x2){rcp(d[0]), rcp(d[1])}); }
      if (c & 1) {
#pragma unroll
        for (int m = 0; m < 2; ++m) { auto r0 = __builtin_amdgcn_permlane32_swap(yw[m][0], yw[m][2], false, false), r1 = __builtin_amdgcn_permlane32_swap(yw[m][1], yw[m][3], false, false);
          st16_dev(wr, ho + ((7 * cb4 + odd + c - 1) >> 1) * 2048 + m * 1024, (u32x4){r0[0], r1[0], r0[1], r1[1]}); } }
      if (c == 6) { auto r0 = __builtin_amdgcn_permlane32_swap(yw[0][0], yw[1][0], false, false), r1 = __builtin_amdgcn_permlane32_swap(yw[0][1], yw[1][1], false, false);
        st16_dev(wr, ho + ((7 * cb4 + 6 - 6 * odd) >> 1) * 2048 + 512 * odd + (t2 & 32) * 16, (u32x4){r0[0], r1[0], r0[1], r1[1]}); } }
    arrive(ws, C_U + rb4 * 2 + (cb4 >> 4));
  }
  {
    LANES();
    f32x16 keep[2];
    f32x16 acc[4];
    { G5();
      unsigned va = lane * 16 + wk * 1024, v2 = va ^ 1024, vb = lbase + wk * 8448 + ((lane >> 2) & 7) * 1056 + h * 512 + 8 * (lane & 3);
      unsigned n0 = g0, n1 = g0 + 32 * 2048, n2 = g0 + 64 * 2048, n3 = g0 + 96 * 2048;
      const char* ap = ws + WS_HH + (size_t)rg2 * (F * 64) + kh * (56 * 2048) + wk * 2048;
      asm volatile("" : "+v"(va), "+v"(v2), "+v"(vb), "+v"(n0), "+v"(n1), "+v"(n2), "+v"(n3), "+s"(ap));
      asm volatile(K5_PRE1 :: "s"(w2), "s"(F * 2048), "s"(lbase + wv * 1056), "s"(0), "v"(n0), "v"(n1), "v"(n2), "v"(n3) : K5_PRECLOB);
      if (wv == 0) wait_ge(flagp(ws, C_U + (rbk2 >> 1) * 2 + kh, xcd), 16);
      BAR();
      asm volatile(K5_ASM : K5_OUT(acc, keep) : "s"(ap), "s"(56 * 2048 - 2048 * wk), "s"(w2), "s"(F * 2048),
                   "s"(lbase + wv * 1056), "s"(0), "v"(va), "v"(vb), "v"(n0), "v"(n1), "v"(n2), "v"(n3), "v"(v2) : K5_CLOB); }
    const float* gl = (const float*)(smem + TB) + 192;
    char* sp = smem + wk * 3 * K5_SS + wm * 8192 + lane * 16;
#pragma unroll
    for (int i = 0; i < 4; ++i) *(f32x4*)(sp + i * 1024) = (f32x4){acc[3][4 * i], acc[3][4 * i + 1], acc[3][4 * i + 2], acc[3][4 * i + 3]};
    BAR();
    const char* rp = smem + (wk ^ 1) * 3 * K5_SS + wm * 8192 + lane * 16;
    { u32x4 s[2];
#pragma unroll
      for (int i = 0; i < 4; ++i) { const f32x4 pv = *(const f32x4*)(rp + i * 1024);
#pragma unroll
        for (int e = 0; e < 2; ++e) s[i >> 1][2 * (i & 1) + e] = pkv(PAIR(acc[1], 4 * i + 2 * e) + PAIR(pv, 2 * e)); }
      const unsigned xo = WS_XCH + blockIdx.x * 16384 + wv * 2048 + lane * 16;
      st16_dev(wr, xo, s[0]); st16_dev(wr, xo + 1024, s[1]); }
    asm volatile(K5_DEF : K5_DEFIO(acc) : K5_DEFIN(keep), "v"(lbase + wk * 8448 + ((lane >> 2) & 7) * 1056 + h * 512 + 8 * (lane & 3)) : K5_DEFCLOB);
    u32x4 pr[2];
    auto xoff = [&](int m) { return WS_XCH + (blockIdx.x ^ 1) * 16384 + wv * 2048 + m * 1024 + lane * 16; };
    pr[0] = ld16_dev(wr, xoff(0)); pr[1] = ld16_dev(wr, xoff(1));
    FENCE();
#pragma unroll
    for (int i = 0; i < 4; ++i) *(f32x4*)(sp + 4096 + i * 1024) = (f32x4){acc[2][4 * i], acc[2][4 * i + 1], acc[2][4 * i + 2], acc[2][4 * i + 3]};
    BAR();
    f32x4 gv[4]; f32x2 y[2][4];
#pragma unroll
    for (int i = 0; i < 4; ++i) { const f32x4 pv = *(const f32x4*)(rp + 4096 + i * 1024); gv[i] = *(const f32x4*)(gl + 16 * i + 4 * g);
#pragma unroll
      for (int e = 0; e < 2; ++e) { const int p = i >> 1, d = 2 * (i & 1) + e;
        y[p][d] = fma2(PAIR(gv[i], 2 * e), PAIR(acc[0], 4 * i + 2 * e) + PAIR(pv, 2 * e), up2(x1[p][d])); asm volatile("" : "+v"(y[p][d])); } }
    FENCE();
    ld_again<2>(pr, wr, xoff);
    const rsrc_t orr = mkrsrc(out, S * D * 2);
    u32x4 ov[2];
#pragma unroll
    for (int m = 0; m < 2; ++m) { u32x4 o;
#pragma unroll
      for (int d = 0; d < 4; ++d) o[d] = pkv(fma2(PAIR(gv[2 * m + (d >> 1)], 2 * (d & 1)), up2(pr[m][d]), y[m][d]));
      auto r0 = __builtin_amdgcn_permlane16_swap(o[0], o[2], false, false), r1 = __builtin_amdgcn_permlane16_swap(o[1], o[3], false, false);
      ov[m] = (u32x4){r0[0], r1[0], r0[1], r1[1]}; }
    const unsigned oo = ((rg2 * 32 + wk * 16 + (lane & 15)) * D + cb2 * 64 + 16 * (g & 1) + 8 * (g >> 1)) * 2;
    __builtin_amdgcn_raw_buffer_store_b128(ov[0], orr, oo, 0, 17); __builtin_amdgcn_raw_buffer_store_b128(ov[1], orr, oo + 64, 0, 17);
  }
}

extern "C" void kernel_launch(void* const* d_in, const int* in_sizes, int n_in, void* d_out, int out_size, void* d_ws, size_t ws_size,
                              hipStream_t stream) {
  const u16* x = (const u16*)d_in[0]; const u16* wqkv = (const u16*)d_in[1]; const u16* wphi = (const u16*)d_in[2]; const u16* wo = (const u16*)d_in[3];
  const u16* w1 = (const u16*)d_in[5]; const u16* w3 = (const u16*)d_in[6]; const u16* w2 = (const u16*)d_in[7];
  const u16* c = (const u16*)d_in[8]; const u16* wmod = (const u16*)d_in[9];
  char* ws = (char*)d_ws;
  k_main<<<256, 512, 0, stream>>>(x, wqkv, wphi, wo, w1, w3, w2, c, wmod, ws, (u16*)d_out);
}
```
